```python
import torch

def _softmax(x, axis=-1):
    return torch.softmax(x, dim=axis)
def _gelu(x):
    return torch.nn.functional.gelu(x, approximate="tanh")
def _silu(x):
    return torch.nn.functional.silu(x)
def _sigmoid(x):
    return torch.sigmoid(x)
def _layernorm(x, axis=-1, eps=1e-5):
    m = x.mean(dim=axis, keepdim=True)
    v = x.var(dim=axis, keepdim=True, unbiased=False)
    return (x - m) / torch.sqrt(v + eps)
def _scatter_add(tgt, idx, src, axis=0):
    return tgt.clone().index_add_(axis, idx, src)
def _segment_sum(src, seg, n):
    out = torch.zeros((n,) + src.shape[1:], dtype=src.dtype)
    return out.index_add_(0, seg, src)
def _pad(x, pw):
    flat = tuple(v for pair in reversed(pw) for v in pair)
    return torch.nn.functional.pad(x, flat)

kernel_name = 'sequential_preLN__mha_block__glu_gated_glu__prompt'

def setup_inputs():

    _w = (lambda a: (lambda t: t.to(torch.bfloat16) if t.is_floating_point() else t)(torch.as_tensor(a)))
    def _A_setup():
        B, S, D, H = 1, 2048, 512, 32
        return {
            "x": _w((lambda *s: torch.randn(*s, dtype=torch.bfloat16) * 0.05)(B, S, D)),
            "wqkv": _w((lambda *s: torch.randn(*s, dtype=torch.bfloat16) * 0.05)(D, 3 * D)),
            "wo": _w((lambda *s: torch.randn(*s, dtype=torch.bfloat16) * 0.05)(D, D)),
        }
    _A_in = _A_setup()

    def _B_setup():
        B, S, D, F = 1, 2048, 512, 1376
        return {
            "x": _w((lambda *s: torch.randn(*s, dtype=torch.bfloat16) * 0.05)(B, S, D)),
            "w_gate": _w((lambda *s: torch.randn(*s, dtype=torch.bfloat16) * 0.05)(D, F)),
            "w_up": _w((lambda *s: torch.randn(*s, dtype=torch.bfloat16) * 0.05)(D, F)),
            "w_down": _w((lambda *s: torch.randn(*s, dtype=torch.bfloat16) * 0.05)(F, D)),
        }
    _B_in = _B_setup()

    return {
        "A_x": _A_in["x"],
        "A_wqkv": _A_in["wqkv"],
        "A_wo": _A_in["wo"],
        "B_x": _B_in["x"],
        "B_w_gate": _B_in["w_gate"],
        "B_w_up": _B_in["w_up"],
        "B_w_down": _B_in["w_down"],
    }

def reference(A_x, A_wqkv, A_wo, B_x, B_w_gate, B_w_up, B_w_down):
    def _A_ref(x, wqkv, wo):
        B, S, D = x.shape
        H = 32
        Dh = D // H
        qkv = (lambda x, *d: x.permute(*d))((x @ wqkv).reshape(B, S, 3, H, Dh), 2, 0, 3, 1, 4)
        q, k, v = qkv[0], qkv[1], qkv[2]
        scores = torch.einsum("bhqd,bhkd->bhqk", q, k) * (Dh ** -0.5)
        p = _softmax(scores, axis=-1)
        o = torch.einsum("bhqk,bhkd->bhqd", p, v)
        return (lambda x, *d: x.permute(*d))(o, 0, 2, 1, 3).reshape(B, S, D) @ wo

    def _B_ref(x, w_gate, w_up, w_down):
        g = torch.einsum("bsd,df->bsf", x, w_gate)
        u = torch.einsum("bsd,df->bsf", x, w_up)
        h = _sigmoid(g) * u
        return torch.einsum("bsf,fd->bsd", h, w_down)

    x = A_x
    h = x + _A_ref(_layernorm(x, axis=-1), A_wqkv, A_wo)
    h2 = h + _B_ref(_layernorm(h, axis=-1), B_w_gate, B_w_up, B_w_down)
    return h2
```

```cpp
#include <hip/hip_runtime.h>
#include <hip/hip_bf16.h>
typedef unsigned short u16;
__device__ __forceinline__ float bf2f(u16 b) { return __uint_as_float((unsigned)b << 16); }
__device__ __forceinline__ u16 f2bf(float f) { unsigned u = __float_as_uint(f); return (u16)((u + 0x7fffu + ((u >> 16) & 1u)) >> 16); }
constexpr int S = 2048, D = 512, H = 32, DH = 16, F = 1376;

__global__ void ln_k(const u16* x, u16* y) {
  int row = blockIdx.x; int t = threadIdx.x;
  float v[8]; float s = 0;
  for (int j = 0; j < 8; ++j) { v[j] = bf2f(x[row * D + t * 8 + j]); s += v[j]; }
  for (int o = 1; o < 64; o <<= 1) s += __shfl_xor(s, o);
  float m = s / D; float q = 0;
  for (int j = 0; j < 8; ++j) { v[j] -= m; q += v[j] * v[j]; }
  for (int o = 1; o < 64; o <<= 1) q += __shfl_xor(q, o);
  float r = rsqrtf(q / D + 1e-5f);
  for (int j = 0; j < 8; ++j) y[row * D + t * 8 + j] = f2bf(v[j] * r);
}
__global__ void gemm_k(const u16* A, const u16* W, const u16* res, u16* C, int N, int K) {
  int n = blockIdx.x * 64 + threadIdx.x; int m0 = blockIdx.y * 8;
  if (n >= N) return;
  float acc[8] = {};
  for (int k = 0; k < K; ++k) { float w = bf2f(W[(size_t)k * N + n]); for (int i = 0; i < 8; ++i) acc[i] += bf2f(A[(size_t)(m0 + i) * K + k]) * w; }
  for (int i = 0; i < 8; ++i) { float v = bf2f(f2bf(acc[i])); if (res) v += bf2f(res[(size_t)(m0 + i) * N + n]); C[(size_t)(m0 + i) * N + n] = f2bf(v); }
}
__global__ void attn_k(const u16* qkv, u16* o) {
  int h = blockIdx.y; int q = blockIdx.x * 64 + threadIdx.x;
  float qv[16]; for (int d = 0; d < 16; ++d) qv[d] = bf2f(qkv[(size_t)q * 1536 + h * 16 + d]);
  float m = -1e30f, l = 0, acc[16] = {};
  for (int k = 0; k < S; ++k) {
    float s = 0; for (int d = 0; d < 16; ++d) s += qv[d] * bf2f(qkv[(size_t)k * 1536 + 512 + h * 16 + d]);
    s *= 0.25f;
    float mn = fmaxf(m, s); float a = __expf(m - mn), p = __expf(s - mn);
    l = l * a + p; for (int d = 0; d < 16; ++d) acc[d] = acc[d] * a + p * bf2f(qkv[(size_t)k * 1536 + 1024 + h * 16 + d]);
    m = mn;
  }
  for (int d = 0; d < 16; ++d) o[(size_t)q * D + h * 16 + d] = f2bf(acc[d] / l);
}
__global__ void glu_k(const u16* g, const u16* u, u16* a, int n) {
  int i = blockIdx.x * 256 + threadIdx.x; if (i >= n) return;
  float gv = bf2f(g[i]); float s = bf2f(f2bf(1.f / (1.f + __expf(-gv)))); a[i] = f2bf(s * bf2f(u[i]));
}
__global__ void empty_k(int* p) { if (p == nullptr) return; }
extern "C" void kernel_launch(void* const* d_in, const int* in_sizes, int n_in, void* d_out, int out_size, void* d_ws, size_t ws_size, hipStream_t stream) {
  const u16* x = (const u16*)d_in[0]; const u16* wqkv = (const u16*)d_in[1]; const u16* wo = (const u16*)d_in[2];
  const u16* wg = (const u16*)d_in[4]; const u16* wu = (const u16*)d_in[5]; const u16* wd = (const u16*)d_in[6];
  u16* ws = (u16*)d_ws; u16* ln = ws; u16* qkv = ln + S * D; u16* o = qkv + S * 1536; u16* h = o + S * D; u16* g = h + S * D; u16* u = g + S * F; u16* a = u + S * F;
  ln_k<<<S, 64, 0, stream>>>(x, ln);
  gemm_k<<<dim3(1536 / 64, S / 8), 64, 0, stream>>>(ln, wqkv, nullptr, qkv, 1536, D);
  attn_k<<<dim3(S / 64, H), 64, 0, stream>>>(qkv, o);
  gemm_k<<<dim3(D / 64, S / 8), 64, 0, stream>>>(o, wo, x, h, D, D);
  ln_k<<<S, 64, 0, stream>>>(h, ln);
  gemm_k<<<dim3((F + 63) / 64, S / 8), 64, 0, stream>>>(ln, wg, nullptr, g, F, D);
  gemm_k<<<dim3((F + 63) / 64, S / 8), 64, 0, stream>>>(ln, wu, nullptr, u, F, D);
  glu_k<<<(S * F + 255) / 256, 256, 0, stream>>>(g, u, a, S * F);
  gemm_k<<<dim3(D / 64, S / 8), 64, 0, stream>>>(a, wd, h, (u16*)d_out, D, F);
}
```

```cpp
#include <hip/hip_runtime.h>
#include <hip/hip_bf16.h>

typedef unsigned short u16;
typedef unsigned long long u64;
typedef __bf16 bf16x8 __attribute__((ext_vector_type(8)));
typedef __bf16 bf16x16 __attribute__((ext_vector_type(16)));
typedef __bf16 bf16x2 __attribute__((ext_vector_type(2)));
typedef short s16x4 __attribute__((ext_vector_type(4)));
typedef float f32x2 __attribute__((ext_vector_type(2)));
typedef float f32x4 __attribute__((ext_vector_type(4)));
typedef float f32x8 __attribute__((ext_vector_type(8)));
typedef float f32x16 __attribute__((ext_vector_type(16)));
typedef unsigned u32x2 __attribute__((ext_vector_type(2)));
typedef unsigned u32x4 __attribute__((ext_vector_type(4)));
#define LAS __attribute__((address_space(3)))
#define GAS __attribute__((address_space(1)))
typedef LAS char* lptr;
#define DI __device__ __forceinline__

constexpr int S = 2048, D = 512, NH = 32, FF = 1376;
constexpr float LOG2E = 1.4426950408889634f;
constexpr float QSCALE = 0.25f * LOG2E;
constexpr float LN_EPS = 1e-5f;
constexpr size_t MiB = 1u << 20;
constexpr size_t WS_FLAG = 0;
constexpr size_t WS_KMAX = 64 * 1024;
constexpr size_t WS_STAT = 128 * 1024;
constexpr size_t WS_Q = 1 * MiB, WS_K = 3 * MiB, WS_V = 5 * MiB, WS_O = 7 * MiB, WS_H = 9 * MiB, WS_ACT = 11 * MiB;
constexpr size_t WS_END = 17 * MiB;
constexpr int LDS_BYTES = 160 * 1024;

#define MFMA16(a, b, c) __builtin_amdgcn_mfma_f32_16x16x32_bf16((a), (b), (c), 0, 0, 0)
#define MFMA32(a, b, c) __builtin_amdgcn_mfma_f32_32x32x16_bf16((a), (b), (c), 0, 0, 0)
#define SBAR() __builtin_amdgcn_sched_barrier(0)
#define WAIT_BAR(N) asm volatile("s_waitcnt vmcnt(" #N ") lgkmcnt(0)\n\ts_barrier" ::: "memory")
#define LGKM0() asm volatile("s_waitcnt lgkmcnt(0)" ::: "memory")

DI void dma16(unsigned voff, const void* sbase, unsigned lds) {
  unsigned keep;
  asm volatile("s_mov_b32 %0, m0\n\ts_mov_b32 m0, %3\n\ts_nop 0\n\tglobal_load_lds_dwordx4 %1, %2\n\ts_mov_b32 m0, %0"
               : "=&s"(keep) : "v"(voff), "s"(sbase), "s"(lds) : "memory");
}
DI void dma16_sc1(unsigned voff, const void* sbase, unsigned lds) {
  unsigned keep;
  asm volatile("s_mov_b32 %0, m0\n\ts_mov_b32 m0, %3\n\ts_nop 0\n\tglobal_load_lds_dwordx4 %1, %2 sc1\n\ts_mov_b32 m0, %0"
               : "=&s"(keep) : "v"(voff), "s"(sbase), "s"(lds) : "memory");
}
DI void dma16v(const void* vaddr, unsigned lds) {
  unsigned keep;
  asm volatile("s_mov_b32 %0, m0\n\ts_mov_b32 m0, %2\n\ts_nop 0\n\tglobal_load_lds_dwordx4 %1, off\n\ts_mov_b32 m0, %0"
               : "=&s"(keep) : "v"(vaddr), "s"(lds) : "memory");
}
typedef short v4i16_t __attribute__((ext_vector_type(4)));
DI s16x4 trd(lptr p) { return __builtin_bit_cast(s16x4, __builtin_amdgcn_ds_read_tr16_b64_v4i16((LAS v4i16_t*)p)); }
DI bf16x8 trd2(lptr p0, lptr p1) { const s16x4 a = trd(p0), b = trd(p1); return __builtin_bit_cast(bf16x8, __builtin_shufflevector(a, b, 0, 1, 2, 3, 4, 5, 6, 7)); }
DI bf16x8 lrd(lptr p) { return *(LAS bf16x8*)p; }
DI unsigned pk(float lo, float hi) { f32x2 v = {lo, hi}; return __builtin_bit_cast(unsigned, __builtin_convertvector(v, bf16x2)); }
DI float bflo(unsigned u) { return __uint_as_float(u << 16); }
DI float bfhi(unsigned u) { return __uint_as_float(u & 0xffff0000u); }
DI bf16x8 ones8() { const u32x4 o = {0x3f803f80u, 0x3f803f80u, 0x3f803f80u, 0x3f803f80u}; return __builtin_bit_cast(bf16x8, o); }
DI u32x4 swap32(unsigned a0, unsigned a1, unsigned b0, unsigned b1) {
  const auto r0 = __builtin_amdgcn_permlane32_swap(a0, b0, false, false);
  const auto r1 = __builtin_amdgcn_permlane32_swap(a1, b1, false, false);
  return (u32x4){r0[0], r1[0], r0[1], r1[1]};
}
DI float xor16(float v) { return __shfl_xor(v, 16); }
DI float xor32(float v) { return __shfl_xor(v, 32); }

__global__ __launch_bounds__(512, 2) void k_qkv(const u16* __restrict__ x, const u16* __restrict__ wqkv, char* __restrict__ ws) {
  extern __shared__ __attribute__((aligned(16))) char smem[];
  const int b = blockIdx.x, nq = b & 7, tt = b >> 3;
  const int lane = threadIdx.x & 63, w = __builtin_amdgcn_readfirstlane(threadIdx.x >> 6);
  const int m = w & 1, hl = w >> 1, t = lane & 15, g = lane >> 4, q = t >> 2, p = lane & 3;
  const unsigned lds0 = (unsigned)(size_t)(lptr)smem;
  if (w == 0 && lane < 4) ((unsigned*)(ws + WS_FLAG))[b * 4 + lane] = 0u;
  const unsigned xoff = (unsigned)((tt * 64 + 8 * w + (lane >> 3)) * 1024 + (((lane & 7) ^ ((4 * w + (lane >> 4)) & 7)) * 16));
  unsigned woff[3];
#pragma unroll
  for (int n = 0; n < 3; ++n) {
    const int l24 = 8 * n + (lane >> 3), row = 8 * w + l24 / 3, mtx = l24 % 3;
    const int f = ((row >> 1) & 1) | (((row >> 3) & 1) << 1);
    woff[n] = (unsigned)(row * 3072 + mtx * 1024 + nq * 128 + (((((lane & 7) >> 1) ^ f) << 1) | (lane & 1)) * 16);
  }
#define QKV_ISSUE(kc, slot) do { const unsigned sb_ = lds0 + (slot) * 32768; \
    dma16(xoff, (const char*)x + (kc) * 128, sb_ + w * 1024); \
    _Pragma("unroll") for (int n_ = 0; n_ < 3; ++n_) dma16(woff[n_], (const char*)wqkv + (kc) * (64 * 3072), sb_ + 8192 + (3 * w + n_) * 1024); } while (0)
  QKV_ISSUE(0, 0); QKV_ISSUE(1, 1); QKV_ISSUE(2, 2); QKV_ISSUE(3, 3); QKV_ISSUE(4, 4);
  const lptr xa0 = (lptr)smem + (m * 32 + t) * 128 + ((g ^ (t >> 1)) * 16);
  const lptr xa1 = (lptr)smem + (m * 32 + t) * 128 + (((g ^ (t >> 1)) ^ 4) * 16);
  const lptr wa = (lptr)smem + 8192 + (8 * g + q) * 384 + ((hl ^ (((q >> 1) & 1) | ((g & 1) << 1))) * 32) + p * 8;
  f32x4 acc[3][2] = {}, cs[3] = {}, sx[2] = {}, sq[2] = {};
  const bf16x8 one = ones8();
#define QKV_CHUNK(kc, slot) do { \
    _Pragma("unroll") for (int s_ = 0; s_ < 2; ++s_) { \
      const lptr xp_ = (s_ ? xa1 : xa0) + (slot) * 32768; const lptr wp_ = wa + (slot) * 32768 + s_ * (32 * 384); \
      const bf16x8 x0_ = lrd(xp_), x1_ = lrd(xp_ + 2048); \
      const bf16x8 wq_ = trd2(wp_, wp_ + 4 * 384), wk_ = trd2(wp_ + 128, wp_ + 128 + 4 * 384), wv_ = trd2(wp_ + 256, wp_ + 256 + 4 * 384); \
      acc[0][0] = MFMA16(wq_, x0_, acc[0][0]); acc[0][1] = MFMA16(wq_, x1_, acc[0][1]); \
      acc[1][0] = MFMA16(wk_, x0_, acc[1][0]); acc[1][1] = MFMA16(wk_, x1_, acc[1][1]); \
      acc[2][0] = MFMA16(x0_, wv_, acc[2][0]); acc[2][1] = MFMA16(x1_, wv_, acc[2][1]); \
      if (m == s_) { cs[0] = MFMA16(wq_, one, cs[0]); cs[1] = MFMA16(wk_, one, cs[1]); cs[2] = MFMA16(one, wv_, cs[2]); } \
      if (hl == ((2 * (kc) + s_) & 3)) { sx[0] = MFMA16(one, x0_, sx[0]); sx[1] = MFMA16(one, x1_, sx[1]); sq[0] = MFMA16(x0_, x0_, sq[0]); sq[1] = MFMA16(x1_, x1_, sq[1]); } \
    } } while (0)
  WAIT_BAR(16); QKV_CHUNK(0, 0);
  WAIT_BAR(12); QKV_ISSUE(5, 0); QKV_CHUNK(1, 1);
  WAIT_BAR(12); QKV_ISSUE(6, 1); QKV_CHUNK(2, 2);
  WAIT_BAR(12); QKV_ISSUE(7, 2); QKV_CHUNK(3, 3);
  WAIT_BAR(12); QKV_CHUNK(4, 4);
  WAIT_BAR(8); QKV_CHUNK(5, 0);
  WAIT_BAR(4); QKV_CHUNK(6, 1);
  WAIT_BAR(0); QKV_CHUNK(7, 2);
#undef QKV_ISSUE
#undef QKV_CHUNK
  LAS float* rs = (LAS float*)((lptr)smem + 4 * 32768);
  LAS float* cp = rs + 512;
  LAS float* mr = cp + 384 + w * 64;
#pragma unroll
  for (int sub = 0; sub < 2; ++sub) {
    const int tok = m * 32 + sub * 16 + t;
    if (g == 0) rs[(hl * 64 + tok) * 2] = sx[sub][0];
    const float dg = (t & 2) ? ((t & 1) ? sq[sub][3] : sq[sub][2]) : ((t & 1) ? sq[sub][1] : sq[sub][0]);
    if (g == q) rs[(hl * 64 + tok) * 2 + 1] = dg;
  }
  if (t == 0) { *(LAS f32x4*)(cp + ((m * 4 + hl) * 3 + 0) * 16 + 4 * g) = cs[0]; *(LAS f32x4*)(cp + ((m * 4 + hl) * 3 + 1) * 16 + 4 * g) = cs[1]; }
  if (g == 0) cp[((m * 4 + hl) * 3 + 2) * 16 + t] = cs[2][0];
  WAIT_BAR(0);
  float mean[2], rstd[2];
#pragma unroll
  for (int sub = 0; sub < 2; ++sub) {
    const int tok = m * 32 + sub * 16 + t; float s1 = 0.f, s2 = 0.f;
#pragma unroll
    for (int hh = 0; hh < 4; ++hh) { const f32x2 v = *(LAS f32x2*)(rs + (hh * 64 + tok) * 2); s1 += v[0]; s2 += v[1]; }
    mean[sub] = s1 * (1.f / D);
    rstd[sub] = __builtin_amdgcn_rsqf(fmaxf(s2 * (1.f / D) - mean[sub] * mean[sub], 0.f) + LN_EPS);
    if (g == 0) *(LAS f32x2*)(mr + (sub * 16 + t) * 2) = (f32x2){mean[sub], rstd[sub]};
  }
  const f32x4 cq = *(LAS f32x4*)(cp + (hl * 3 + 0) * 16 + 4 * g) + *(LAS f32x4*)(cp + ((4 + hl) * 3 + 0) * 16 + 4 * g);
  const f32x4 ck = *(LAS f32x4*)(cp + (hl * 3 + 1) * 16 + 4 * g) + *(LAS f32x4*)(cp + ((4 + hl) * 3 + 1) * 16 + 4 * g);
  const float cv = cp[(hl * 3 + 2) * 16 + t] + cp[((4 + hl) * 3 + 2) * 16 + t];
  const int head = nq * 4 + hl, T = tt * 2 + m;
  const size_t tbase = (size_t)(head * 64 + T) * 1024;
  const int r32 = t + 16 * (lane >> 5), c = g & 1;
  {
    f32x4 y[2];
#pragma unroll
    for (int sub = 0; sub < 2; ++sub) y[sub] = (acc[0][sub] - mean[sub] * cq) * (rstd[sub] * QSCALE);
    *(u32x4*)(ws + WS_Q + tbase + (c * 32 + r32) * 16) = swap32(pk(y[0][0], y[0][1]), pk(y[0][2], y[0][3]), pk(y[1][0], y[1][1]), pk(y[1][2], y[1][3]));
  }
  {
    f32x4 y[2]; float n2[2];
#pragma unroll
    for (int sub = 0; sub < 2; ++sub) { y[sub] = (acc[1][sub] - mean[sub] * ck) * rstd[sub];
      n2[sub] = (y[sub][0] * y[sub][0] + y[sub][1] * y[sub][1]) + (y[sub][2] * y[sub][2] + y[sub][3] * y[sub][3]);
      n2[sub] += xor16(n2[sub]); n2[sub] += xor32(n2[sub]); }
    const int rho = (r32 & 0x13) | ((r32 & 4) << 1) | ((r32 & 8) >> 1);
    *(u32x4*)(ws + WS_K + tbase + (c * 32 + rho) * 16) = swap32(pk(y[0][0], y[0][1]), pk(y[0][2], y[0][3]), pk(y[1][0], y[1][1]), pk(y[1][2], y[1][3]));
    float mx = fmaxf(n2[0], n2[1]);
#pragma unroll
    for (int o = 1; o < 16; o <<= 1) mx = fmaxf(mx, __shfl_xor(mx, o));
    if (lane == 0) ((float*)(ws + WS_KMAX))[head * 64 + T] = mx;
  }
  {
    LGKM0();
    f32x4 y[2];
#pragma unroll
    for (int sub = 0; sub < 2; ++sub) {
      const f32x4 a = *(LAS f32x4*)(mr + (sub * 16 + 4 * g) * 2), bb = *(LAS f32x4*)(mr + (sub * 16 + 4 * g + 2) * 2);
      y[sub][0] = (acc[2][sub][0] - a[0] * cv) * a[1]; y[sub][1] = (acc[2][sub][1] - a[2] * cv) * a[3];
      y[sub][2] = (acc[2][sub][2] - bb[0] * cv) * bb[1]; y[sub][3] = (acc[2][sub][3] - bb[2] * cv) * bb[3];
    }
    *(u32x4*)(ws + WS_V + tbase + ((lane >> 5) * 2 + c) * 256 + t * 16) = swap32(pk(y[0][0], y[0][1]), pk(y[0][2], y[0][3]), pk(y[1][0], y[1][1]), pk(y[1][2], y[1][3]));
  }
}

constexpr int AT_K = 0, AT_V = 65536, AT_ONE = 131072, AT_FLAG = 131072 + 8192;
DI f32x16 smf(bf16x8 a, f32x8 pb, f32x16 c, int idx) { return __builtin_amdgcn_smfmac_f32_32x32x32_bf16(a, __builtin_bit_cast(bf16x16, pb), c, idx, 0, 0); }
DI f32x8 lo8(f32x16 v) { return __builtin_shufflevector(v, v, 0, 1, 2, 3, 4, 5, 6, 7); }
DI f32x8 hi8(f32x16 v) { return __builtin_shufflevector(v, v, 8, 9, 10, 11, 12, 13, 14, 15); }

template <bool FUSED>
DI void attn_phase(char* smem, char* __restrict__ ws, int head, int qt, int w, int lane) {
  const unsigned lds0 = (unsigned)(size_t)(lptr)smem;
  const char* kg = ws + WS_K + (size_t)head * 65536 + w * 1024;
  const char* vg = ws + WS_V + (size_t)head * 65536 + w * 1024;
  const int T = qt * 8 + w;
  u32x4 qraw; float kmx;
  asm volatile("global_load_dwordx4 %0, %1, %2" : "=v"(qraw) : "v"(lane * 16), "s"(ws + WS_Q + (size_t)(head * 64 + T) * 1024) : "memory");
  asm volatile("global_load_dword %0, %1, %2" : "=v"(kmx) : "v"(lane * 4), "s"(ws + WS_KMAX + head * 256) : "memory");
#define AT_ISSUE(c) do { dma16(lane * 16, kg + (c) * 8192, lds0 + AT_K + (c) * 8192 + w * 1024); dma16(lane * 16, vg + (c) * 8192, lds0 + AT_V + (c) * 8192 + w * 1024); } while (0)
  AT_ISSUE(0); AT_ISSUE(1);
  *(LAS bf16x8*)((lptr)smem + AT_ONE + threadIdx.x * 16) = ones8();
  asm volatile("s_waitcnt vmcnt(4)" : "+v"(qraw), "+v"(kmx) :: "memory");
  float n2 = 0.f;
#pragma unroll
  for (int i = 0; i < 4; ++i) { const float a = bflo(qraw[i]), bb = bfhi(qraw[i]); n2 = fmaf(a, a, fmaf(bb, bb, n2)); }
  n2 += xor32(n2);
#pragma unroll
  for (int o = 1; o < 64; o <<= 1) kmx = fmaxf(kmx, __shfl_xor(kmx, o));
  const float bound = __builtin_sqrtf(n2 * kmx) * 1.01f;
  const bool unsafe = !(bound <= 40.f);
  if (lane == 0) *(LAS unsigned*)((lptr)smem + AT_FLAG + w * 4) = 0u;
  if (unsafe) *(LAS unsigned*)((lptr)smem + AT_FLAG + w * 4) = 1u;
  WAIT_BAR(2);
  const u32x4 fa = *(LAS u32x4*)((lptr)smem + AT_FLAG), fb = *(LAS u32x4*)((lptr)smem + AT_FLAG + 16);
  const bool slow = __builtin_amdgcn_readfirstlane(fa[0] | fa[1] | fa[2] | fa[3] | fb[0] | fb[1] | fb[2] | fb[3]) != 0;
  const bf16x8 qf = __builtin_bit_cast(bf16x8, qraw);
  const int idx = 0xDDDD;
  lptr kp = (lptr)smem + AT_K + lane * 16;
  const bool vl = (lane & 16) == 0;
  lptr vp = (lptr)smem + (vl ? AT_V : AT_ONE) + (lane >> 5) * 256 + (lane & 15) * 16;
  const int vstep = vl ? 8192 : 0;
  f32x16 o = {};
  if (!slow) {
    f32x16 negm;
#pragma unroll
    for (int i = 0; i < 16; ++i) negm[i] = -bound;
    asm volatile("" : "+v"(negm));
    bf16x8 kf = lrd(kp), v0 = lrd(vp), v1 = lrd(vp + 512);
    f32x16 sa = MFMA32(kf, qf, negm), sb;
    kf = lrd(kp + 1024);
#define AT_TILE(SC, SN, tl) do { \
      SN = MFMA32(kf, qf, negm); kf = lrd(kp + ((tl) + 2) * 1024); \
      _Pragma("unroll") for (int i_ = 0; i_ < 8; ++i_) SC[i_] = __builtin_amdgcn_exp2f(SC[i_]); \
      o = smf(v0, lo8(SC), o, idx); v0 = lrd(((tl) == 7 ? vp + vstep : vp + ((tl) + 1) * 1024)); \
      _Pragma("unroll") for (int i_ = 8; i_ < 16; ++i_) SC[i_] = __builtin_amdgcn_exp2f(SC[i_]); \
      o = smf(v1, hi8(SC), o, idx); v1 = lrd(((tl) == 7 ? vp + vstep : vp + ((tl) + 1) * 1024) + 512); } while (0)
    for (int c = 0; c < 8; ++c) {
      AT_TILE(sa, sb, 0); AT_TILE(sb, sa, 1); AT_TILE(sa, sb, 2); AT_TILE(sb, sa, 3);
      WAIT_BAR(0);
      if (c < 6) AT_ISSUE(c + 2);
      AT_TILE(sa, sb, 4); AT_TILE(sb, sa, 5); AT_TILE(sa, sb, 6); AT_TILE(sb, sa, 7);
      kp += 8192; vp += vstep;
    }
#undef AT_TILE
  } else {
    float mrun = -3.0e38f;
    const f32x16 zero = {};
    for (int c = 0; c < 8; ++c) {
      for (int tl = 0; tl < 8; ++tl) {
        if (tl == 4) { WAIT_BAR(0); if (c < 6) AT_ISSUE(c + 2); }
        f32x16 s = MFMA32(lrd(kp + tl * 1024), qf, zero);
        float tm = s[0];
#pragma unroll
        for (int i = 1; i < 16; ++i) tm = fmaxf(tm, s[i]);
        tm = fmaxf(tm, xor32(tm));
        const float mn = fmaxf(mrun, tm), al = __builtin_amdgcn_exp2f(mrun - mn);
        mrun = mn;
#pragma unroll
        for (int i = 0; i < 16; ++i) { s[i] = __builtin_amdgcn_exp2f(s[i] - mn); o[i] *= al; }
        o = smf(lrd(vp + tl * 1024), lo8(s), o, idx);
        o = smf(lrd(vp + tl * 1024 + 512), hi8(s), o, idx);
      }
      kp += 8192; vp += vstep;
    }
  }
#undef AT_ISSUE
  const float inv = 1.0f / o[8];
  const u32x4 ov = {pk(o[0] * inv, o[1] * inv), pk(o[2] * inv, o[3] * inv), pk(o[4] * inv, o[5] * inv), pk(o[6] * inv, o[7] * inv)};
  const auto rs = __builtin_amdgcn_make_buffer_rsrc(ws + WS_O, 0, 2 * MiB, 0x00020000);
  __builtin_amdgcn_raw_buffer_store_b128(ov, rs, (T * 32 + head) * 1024 + lane * 16, 0, FUSED ? 16 : 0);
}

__global__ __launch_bounds__(512, 2) void k_attn(char* __restrict__ ws) {
  extern __shared__ __attribute__((aligned(16))) char smem[];
  const int b = blockIdx.x, lane = threadIdx.x & 63, w = __builtin_amdgcn_readfirstlane(threadIdx.x >> 6);
  attn_phase<false>(smem, ws, (b & 7) * 4 + (b >> 6), (b >> 3) & 7, w, lane);
}

__global__ __launch_bounds__(512, 2) void k_wo(const u16* __restrict__ x, const u16* __restrict__ wo, char* __restrict__ ws) {
  extern __shared__ __attribute__((aligned(16))) char smem[];
  const int b = blockIdx.x, lane = threadIdx.x & 63, w = __builtin_amdgcn_readfirstlane(threadIdx.x >> 6);
  const int gq = b & 7, j = b >> 3, T = gq * 8 + (j >> 2), ct = j & 3;
  const int t = lane & 15, g = lane >> 4, q = t >> 2, p = lane & 3;
  const unsigned lds0 = (unsigned)(size_t)(lptr)smem;
#pragma unroll
  for (int n = 0; n < 4; ++n) dma16(lane * 16, ws + WS_O + (size_t)(T * 32 + w * 4 + n) * 1024, lds0 + 131072 + (w * 4 + n) * 1024);
  {
    const int r4 = lane >> 4, ch = lane & 15;
#pragma unroll
    for (int n = 0; n < 16; ++n) {
      const int row = 4 * n + r4;
      dma16((unsigned)(row * 1024 + ((ch ^ ((row & 7) << 1)) * 16)), (const char*)wo + (size_t)w * 65536 + ct * 256, lds0 + (w * 16 + n) * 1024);
    }
  }
  u32x2 xr[2];
#pragma unroll
  for (int sub = 0; sub < 2; ++sub) xr[sub] = *(const u32x2*)(x + (size_t)(T * 32 + sub * 16 + t) * D + ct * 128 + w * 16 + 4 * g);
  WAIT_BAR(0);
  const lptr wa = (lptr)smem + (16 * (g >> 1) + 4 * (g & 1) + q) * 256 + ((w ^ (4 * (g & 1) + q)) * 32) + p * 8;
  const lptr oa = (lptr)smem + 131072 + (g >> 1) * 1024 + (g & 1) * 512 + t * 16;
  f32x4 acc[2] = {};
#pragma unroll
  for (int s = 0; s < 16; ++s) {
    const bf16x8 wf = trd2(wa + s * 8192, wa + s * 8192 + 2048);
    acc[0] = MFMA16(wf, lrd(oa + s * 2048), acc[0]); acc[1] = MFMA16(wf, lrd(oa + s * 2048 + 256), acc[1]);
  }
  float s1[2], s2[2];
#pragma unroll
  for (int sub = 0; sub < 2; ++sub) {
    acc[sub][0] += bflo(xr[sub][0]); acc[sub][1] += bfhi(xr[sub][0]); acc[sub][2] += bflo(xr[sub][1]); acc[sub][3] += bfhi(xr[sub][1]);
    s1[sub] = (acc[sub][0] + acc[sub][1]) + (acc[sub][2] + acc[sub][3]);
    s2[sub] = (acc[sub][0] * acc[sub][0] + acc[sub][1] * acc[sub][1]) + (acc[sub][2] * acc[sub][2] + acc[sub][3] * acc[sub][3]);
    s1[sub] += xor16(s1[sub]); s1[sub] += xor32(s1[sub]); s2[sub] += xor16(s2[sub]); s2[sub] += xor32(s2[sub]);
  }
  const int r32 = t + 16 * (lane >> 5), c = g & 1;
  *(u32x4*)(ws + WS_H + (size_t)(T * 32 + ct * 8 + w) * 1024 + (c * 32 + r32) * 16) =
      swap32(pk(acc[0][0], acc[0][1]), pk(acc[0][2], acc[0][3]), pk(acc[1][0], acc[1][1]), pk(acc[1][2], acc[1][3]));
  WAIT_BAR(0);
  LAS float* sc = (LAS float*)(lptr)smem;
  if (g < 2) *(LAS f32x2*)(sc + (w * 32 + g * 16 + t) * 2) = g ? (f32x2){s1[1], s2[1]} : (f32x2){s1[0], s2[0]};
  WAIT_BAR(0);
  if (w == 0) {
    float v = 0.f;
#pragma unroll
    for (int i = 0; i < 8; ++i) v += sc[i * 64 + lane];
    ((float*)(ws + WS_STAT))[(T * 32 + (lane >> 1)) * 8 + ct * 2 + (lane & 1)] = v;
  }
}

__global__ __launch_bounds__(512, 2) void k_gu(const u16* __restrict__ wg, const u16* __restrict__ wu, char* __restrict__ ws) {
  extern __shared__ __attribute__((aligned(16))) char smem[];
  const int b = blockIdx.x, lane = threadIdx.x & 63, w = __builtin_amdgcn_readfirstlane(threadIdx.x >> 6);
  const int gq = b & 7, j = b >> 3, T = gq * 8 + w;
  const int G0 = j < 22 ? 3 * j : 66 + 2 * (j - 22); const bool three = j < 22;
  const unsigned lds0 = (unsigned)(size_t)(lptr)smem;
  {
#pragma unroll
    for (int n = 0; n < 3; ++n) {
      const int o = n * 1024 + 16 * lane, row = 64 * w + o / 192, cc = (o % 192) >> 4; int c6 = cc < 6 ? cc : cc - 6; if (!three) c6 &= 3;
      const char* src = (const char*)(cc < 6 ? wg : wu) + (size_t)row * 2752 + G0 * 32 + c6 * 16;
#pragma unroll
      for (int i = 0; i < 4; ++i) dma16v(src + i * (16 * 2752), lds0 + (12 * w + 3 * i + n) * 1024);
    }
  }
  const auto hr = __builtin_amdgcn_make_buffer_rsrc(ws + WS_H, 0, 2 * MiB, 0x00020000);
  u32x4 hf[32];
#pragma unroll
  for (int s = 0; s < 32; ++s) hf[s] = __builtin_amdgcn_raw_buffer_load_b128(hr, T * 32768 + s * 1024 + lane * 16, 0, 0);
  const int r = lane & 31, h = lane >> 5;
  const f32x4 st0 = *(const f32x4*)(ws + WS_STAT + (size_t)(T * 32 + r) * 32), st1 = *(const f32x4*)(ws + WS_STAT + (size_t)(T * 32 + r) * 32 + 16);
  WAIT_BAR(34);
  const lptr wa = (lptr)smem + (4 * h + ((lane & 15) >> 2)) * 192 + ((lane >> 4) & 1) * 96 + (lane & 3) * 8;
  LAS float* cl = (LAS float*)((lptr)smem + 98304);
  if (w < 3) {
    f32x16 cs = {}; const bf16x8 one = ones8();
#pragma unroll 8
    for (int s = 0; s < 32; ++s) cs = MFMA32(trd2(wa + s * 3072 + w * 32, wa + s * 3072 + w * 32 + 1536), one, cs);
    if (r == 0) {
#pragma unroll
      for (int i = 0; i < 4; ++i) *(LAS f32x4*)(cl + (w * 2 + h) * 16 + 4 * i) = (f32x4){cs[4 * i], cs[4 * i + 1], cs[4 * i + 2], cs[4 * i + 3]};
    }
  }
  f32x16 acc[3] = {};
  if (three) {
#pragma unroll
    for (int s = 0; s < 32; ++s) {
      const bf16x8 hb = __builtin_bit_cast(bf16x8, hf[s]);
#pragma unroll
      for (int t3 = 0; t3 < 3; ++t3) acc[t3] = MFMA32(trd2(wa + s * 3072 + t3 * 32, wa + s * 3072 + t3 * 32 + 1536), hb, acc[t3]);
    }
  } else {
#pragma unroll
    for (int s = 0; s < 32; ++s) {
      const bf16x8 hb = __builtin_bit_cast(bf16x8, hf[s]);
#pragma unroll
      for (int t3 = 0; t3 < 2; ++t3) acc[t3] = MFMA32(trd2(wa + s * 3072 + t3 * 32, wa + s * 3072 + t3 * 32 + 1536), hb, acc[t3]);
    }
  }
  WAIT_BAR(0);
  const float mean = ((st0[0] + st0[2]) + (st1[0] + st1[2])) * (1.f / D);
  const float rstd = __builtin_amdgcn_rsqf(fmaxf(((st0[1] + st0[3]) + (st1[1] + st1[3])) * (1.f / D) - mean * mean, 0.f) + LN_EPS);
  const float ke = -rstd * LOG2E;
#pragma unroll
  for (int t3 = 0; t3 < 3; ++t3) {
    if (t3 < 2 || three) {
      float a[8];
#pragma unroll
      for (int i4 = 0; i4 < 2; ++i4) {
        const f32x4 cg = *(LAS f32x4*)(cl + (t3 * 2 + h) * 16 + 4 * i4), cu = *(LAS f32x4*)(cl + (t3 * 2 + h) * 16 + 8 + 4 * i4);
#pragma unroll
        for (int i = 0; i < 4; ++i) {
          const float gg = fmaf(-mean, cg[i], acc[t3][4 * i4 + i]), uu = fmaf(-mean, cu[i], acc[t3][8 + 4 * i4 + i]);
          a[4 * i4 + i] = (uu * rstd) * __builtin_amdgcn_rcpf(1.f + __builtin_amdgcn_exp2f(gg * ke));
        }
      }
      const int G = G0 + t3;
      *(u32x4*)(ws + WS_ACT + (size_t)((2 * T + (r >> 4)) * 43 + (G >> 1)) * 1024 + ((G & 1) * 2 + h) * 256 + (r & 15) * 16) =
          (u32x4){pk(a[0], a[1]), pk(a[2], a[3]), pk(a[4], a[5]), pk(a[6], a[7])};
    }
  }
}

__global__ __launch_bounds__(512, 2) void k_down(const u16* __restrict__ wd, char* __restrict__ ws, u16* __restrict__ out) {
  extern __shared__ __attribute__((aligned(16))) char smem[];
  const int b = blockIdx.x, lane = threadIdx.x & 63, w = __builtin_amdgcn_readfirstlane(threadIdx.x >> 6);
  const int gq = b & 7, j = b >> 3, T16 = (gq * 4 + (j >> 3)) * 4 + (w & 3), cn = j & 7, kh = w >> 2;
  const int t = lane & 15, g = lane >> 4, q = t >> 2, p = lane & 3;
  const unsigned lds0 = (unsigned)(size_t)(lptr)smem;
  const unsigned woff = (unsigned)((lane >> 3) * 1024 + (((lane & 7) ^ (((lane >> 4) & 3) << 1)) * 16));
  const char* wb = (const char*)wd + cn * 128;
#pragma unroll
  for (int n = 0; n < 20; ++n) {
    const int pc = n * 8 + w, slot = pc >> 2, s = slot < 20 ? slot : slot + 2;
    dma16(woff, wb + (size_t)(s * 32 + (pc & 3) * 8) * 1024, lds0 + pc * 1024);
  }
  const auto ar = __builtin_amdgcn_make_buffer_rsrc(ws + WS_ACT, 0, 128 * 43 * 1024, 0x00020000);
  u32x4 af[22];
#pragma unroll
  for (int i = 0; i < 22; ++i) af[i] = __builtin_amdgcn_raw_buffer_load_b128(ar, (T16 * 43 + kh * 22 + (i < 21 ? i : 21 - kh)) * 1024 + lane * 16, 0, 0);
  u32x2 hres[2];
#pragma unroll
  for (int i = 0; i < 2; ++i) hres[i] = *(const u32x2*)(ws + WS_H + (size_t)((T16 >> 1) * 32 + cn * 4 + kh * 2 + i) * 1024 + ((g & 1) * 32 + (T16 & 1) * 16 + t) * 16 + (g >> 1) * 8);
  WAIT_BAR(24);
  const int x2 = (2 * (g & 1) + (q >> 1)) & 3;
  const lptr wa = (lptr)smem + kh * (20 * 4096) + (16 * (g >> 1) + 4 * (g & 1) + q) * 128 + p * 8;
  f32x4 acc[4] = {};
#define DN_STEP(i, slot) do { const bf16x8 ab_ = __builtin_bit_cast(bf16x8, af[i]); \
    _Pragma("unroll") for (int ct_ = 0; ct_ < 4; ++ct_) { const lptr a_ = wa + (slot) * 4096 + ((ct_ ^ x2) * 32); acc[ct_] = MFMA16(trd2(a_, a_ + 1024), ab_, acc[ct_]); } } while (0)
  DN_STEP(0, 0); DN_STEP(1, 1);
  asm volatile("s_waitcnt lgkmcnt(0)\n\ts_barrier" ::: "memory");
  dma16(woff, wb + (size_t)((20 + (w >> 2)) * 32 + (w & 3) * 8) * 1024, lds0 + w * 1024);
  if (w < 4) dma16(woff, wb + (size_t)(42 * 32 + w * 8) * 1024, lds0 + 20 * 4096 + w * 1024);
#pragma unroll
  for (int i = 2; i < 20; ++i) DN_STEP(i, i);
  WAIT_BAR(0);
  DN_STEP(20, 0);
  if (kh == 0) DN_STEP(21, 1);
#undef DN_STEP
  LAS f32x4* ex = (LAS f32x4*)((lptr)smem + 16384);
  ex[(w * 2 + 0) * 64 + lane] = kh ? acc[0] : acc[2];
  ex[(w * 2 + 1) * 64 + lane] = kh ? acc[1] : acc[3];
  WAIT_BAR(0);
  f32x4 y0 = (kh ? acc[2] : acc[0]) + ex[((w ^ 4) * 2 + 0) * 64 + lane];
  f32x4 y1 = (kh ? acc[3] : acc[1]) + ex[((w ^ 4) * 2 + 1) * 64 + lane];
  y0 += (f32x4){bflo(hres[0][0]), bfhi(hres[0][0]), bflo(hres[0][1]), bfhi(hres[0][1])};
  y1 += (f32x4){bflo(hres[1][0]), bfhi(hres[1][0]), bflo(hres[1][1]), bfhi(hres[1][1])};
  const auto r0 = __builtin_amdgcn_permlane16_swap(pk(y0[0], y0[1]), pk(y1[0], y1[1]), false, false);
  const auto r1 = __builtin_amdgcn_permlane16_swap(pk(y0[2], y0[3]), pk(y1[2], y1[3]), false, false);
  *(u32x4*)(out + (size_t)(T16 * 16 + t) * D + cn * 64 + kh * 32 + (g & 1) * 16 + (g >> 1) * 8) = (u32x4){r0[0], r1[0], r0[1], r1[1]};
}

extern "C" void kernel_launch(void* const* d_in, const int* in_sizes, int n_in, void* d_out, int out_size, void* d_ws, size_t ws_size, hipStream_t stream) {
  const u16* x = (const u16*)d_in[0]; const u16* wqkv = (const u16*)d_in[1]; const u16* wo = (const u16*)d_in[2];
  const u16* wg = (const u16*)d_in[4]; const u16* wu = (const u16*)d_in[5]; const u16* wd = (const u16*)d_in[6];
  char* ws = (char*)d_ws;
  (void)hipFuncSetAttribute((const void*)k_qkv, hipFuncAttributeMaxDynamicSharedMemorySize, LDS_BYTES);
  (void)hipFuncSetAttribute((const void*)k_attn, hipFuncAttributeMaxDynamicSharedMemorySize, LDS_BYTES);
  (void)hipFuncSetAttribute((const void*)k_wo, hipFuncAttributeMaxDynamicSharedMemorySize, LDS_BYTES);
  (void)hipFuncSetAttribute((const void*)k_gu, hipFuncAttributeMaxDynamicSharedMemorySize, LDS_BYTES);
  (void)hipFuncSetAttribute((const void*)k_down, hipFuncAttributeMaxDynamicSharedMemorySize, LDS_BYTES);
  k_qkv<<<256, 512, LDS_BYTES, stream>>>(x, wqkv, ws);
  k_attn<<<256, 512, LDS_BYTES, stream>>>(ws);
  k_wo<<<256, 512, LDS_BYTES, stream>>>(x, wo, ws);
  k_gu<<<256, 512, LDS_BYTES, stream>>>(wg, wu, ws);
  k_down<<<256, 512, LDS_BYTES, stream>>>(wd, ws, (u16*)d_out);
}
```

```cpp
#include <hip/hip_runtime.h>
#include <hip/hip_bf16.h>

typedef unsigned short u16;
typedef unsigned long long u64;
typedef __bf16 bf16x8 __attribute__((ext_vector_type(8)));
typedef __bf16 bf16x2 __attribute__((ext_vector_type(2)));
typedef short s16x4 __attribute__((ext_vector_type(4)));
typedef float f32x2 __attribute__((ext_vector_type(2)));
typedef float f32x4 __attribute__((ext_vector_type(4)));
typedef float f32x8 __attribute__((ext_vector_type(8)));
typedef float f32x16 __attribute__((ext_vector_type(16)));
typedef unsigned u32x2 __attribute__((ext_vector_type(2)));
typedef unsigned u32x4 __attribute__((ext_vector_type(4)));
typedef unsigned u32x32 __attribute__((ext_vector_type(32)));
#define LAS __attribute__((address_space(3)))
#define GAS __attribute__((address_space(1)))
typedef LAS char* lptr;
typedef GAS u64 gu64;
#define DI __device__ __forceinline__
#define RLX_AGENT __ATOMIC_RELAXED, __HIP_MEMORY_SCOPE_AGENT

constexpr int S = 2048, D = 512, FF = 1376;
constexpr float LOG2E = 1.4426950408889634f;
constexpr float QSCALE = 0.25f * LOG2E;
constexpr size_t MiB = 1u << 20;
constexpr size_t WS_EPOCH = 0;
constexpr size_t WS_FLAG = 256 * 1024;
constexpr size_t WS_KMAX = 64 * 1024;
constexpr size_t WS_STAT = 128 * 1024;
constexpr size_t WS_Q = 1 * MiB, WS_K = 3 * MiB, WS_V = 5 * MiB, WS_O = 7 * MiB, WS_H = 9 * MiB, WS_ACT = 11 * MiB;
constexpr size_t WS_END = 17 * MiB;
constexpr int LDS_BYTES = 160 * 1024;
constexpr int FSTR = 128;
constexpr int L_ONE = 131072, L_WG = 0, L_WU = 49152 + 128, L_O = 106496, L_SCR = 152064;

#define WAIT_BAR(N) asm volatile("s_waitcnt vmcnt(" #N ") lgkmcnt(0)\n\ts_barrier" ::: "memory")
#define LGKM_BAR() asm volatile("s_waitcnt lgkmcnt(0)\n\ts_barrier" ::: "memory")

#define P1_HEAD \
"s_mov_b32 %[keep], m0\n\t" \
"s_lshl_b32 s89, s88, 7\n\t" \
"s_mul_i32 s90, s88, 0x30000\n\t" \
"s_add_u32 m0, %[lw], 0x0\n\t" \
"s_nop 0\n\t" \
"buffer_load_dwordx4 v92, s[80:83], s89 offen offset:0 lds\n\t" \
"buffer_load_dwordx4 v93, s[84:87], s90 offen offset:1024 lds\n\t" \
"buffer_load_dwordx4 v93, s[84:87], s90 offen offset:2048 lds\n\t" \
"buffer_load_dwordx4 v93, s[84:87], s90 offen offset:3072 lds\n\t" \
"s_add_u32 s88, s88, 1\n\t" \
"s_and_b32 s88, s88, 7\n\t" \
"s_lshl_b32 s89, s88, 7\n\t" \
"s_mul_i32 s90, s88, 0x30000\n\t" \
"s_add_u32 m0, %[lw], 0x8000\n\t" \
"s_barrier\n\t" \
"buffer_load_dwordx4 v92, s[80:83], s89 offen offset:0 lds\n\t" \
"buffer_load_dwordx4 v93, s[84:87], s90 offen offset:1024 lds\n\t" \
"buffer_load_dwordx4 v93, s[84:87], s90 offen offset:2048 lds\n\t" \
"buffer_load_dwordx4 v93, s[84:87], s90 offen offset:3072 lds\n\t" \
"s_add_u32 s88, s88, 1\n\t" \
"s_and_b32 s88, s88, 7\n\t" \
"s_lshl_b32 s89, s88, 7\n\t" \
"s_mul_i32 s90, s88, 0x30000\n\t"

#define P1_EPI \
"s_mov_b32 vcc_lo, 0xaaaaaaaa\n\t" \
"s_mov_b32 vcc_hi, 0xaaaaaaaa\n\t" \
"s_mov_b32 s88, 0xcccccccc\n\t" \
"s_mov_b32 s89, 0xcccccccc\n\t" \
"s_nop 1\n\t" \
"v_cndmask_b32 v96, v96, v97, vcc\n\t" \
"v_cndmask_b32 v98, v98, v99, vcc\n\t" \
"v_cndmask_b32 v100, v100, v101, vcc\n\t" \
"v_cndmask_b32 v102, v102, v103, vcc\n\t" \
"v_cndmask_b32 v81, v96, v98, s[88:89]\n\t" \
"v_cndmask_b32 v85, v100, v102, s[88:89]\n\t" \
"s_mov_b32 exec_lo, 0x10001\n\t" \
"s_mov_b32 exec_hi, 0x10001\n\t" \
"ds_write_b128 v153, v[24:27]\n\t" \
"ds_write_b128 v153, v[28:31] offset:64\n\t" \
"s_mov_b32 exec_lo, 0xffff\n\t" \
"s_mov_b32 exec_hi, 0x0\n\t" \
"ds_write_b32 v154, v32\n\t" \
"s_mov_b32 exec_lo, 0xf0000f\n\t" \
"s_mov_b32 exec_hi, 0xf0000f00\n\t" \
"ds_write2_b64 v152, v[80:81], v[84:85] offset1:16\n\t" \
"s_mov_b64 exec, -1\n\t" \
"s_waitcnt lgkmcnt(0)\n\t" \
"s_barrier\n\t" \
"ds_read2st64_b64 v[40:43], v155 offset1:1\n\t" \
"ds_read2st64_b64 v[44:47], v155 offset0:2 offset1:3\n\t" \
"ds_read_b128 v[48:51], v156\n\t" \
"ds_read_b128 v[52:55], v156 offset:64\n\t" \
"ds_read_b32 v56, v157\n\t" \
"s_waitcnt lgkmcnt(3)\n\t" \
"v_add_f32 v40, v40, v42\n\t" \
"v_add_f32 v44, v44, v46\n\t" \
"v_add_f32 v41, v41, v43\n\t" \
"v_add_f32 v45, v45, v47\n\t" \
"v_add_f32 v40, v40, v44\n\t" \
"v_add_f32 v41, v41, v45\n\t" \
"v_mul_f32 v40, 0x3b000000, v40\n\t" \
"v_mul_f32 v41, 0x3b000000, v41\n\t" \
"v_fma_f32 v41, -v40, v40, v41\n\t" \
"v_max_f32 v41, 0, v41\n\t" \
"v_add_f32 v41, 0x3727c5ac, v41\n\t" \
"v_rsq_f32 v41, v41\n\t" \
"v_mov_b32 v42, v40\n\t" \
"v_mov_b32 v58, v40\n\t" \
"v_mov_b32 v59, v41\n\t" \
"v_mov_b32 v43, v41\n\t" \
"s_waitcnt lgkmcnt(0)\n\t" \
"ds_write_b64 v158, v[58:59]\n\t" \
"ds_read_b128 v[60:63], v159 offset:0\n\t" \
"ds_read_b128 v[64:67], v159 offset:16\n\t" \
"ds_read_b128 v[68:71], v159 offset:128\n\t" \
"ds_read_b128 v[72:75], v159 offset:144\n\t" \
"v_add_f32 v24, v24, v48\n\t" \
"v_permlane16_swap_b32 v40, v42\n\t" \
"v_permlane16_swap_b32 v41, v43\n\t" \
"v_add_f32 v25, v25, v49\n\t" \
"v_add_f32 v26, v26, v50\n\t" \
"v_add_f32 v27, v27, v51\n\t" \
"v_add_f32 v28, v28, v52\n\t" \
"v_add_f32 v29, v29, v53\n\t" \
"v_add_f32 v30, v30, v54\n\t" \
"v_add_f32 v31, v31, v55\n\t" \
"v_add_f32 v32, v32, v56\n\t" \
"v_mul_f32 v44, 0x3eb8aa3b, v41\n\t" \
"v_mul_f32 v45, 0x3eb8aa3b, v43\n\t" \
"v_fma_f32 v8, -v40, v28, v8\n\t" \
"v_fma_f32 v9, -v40, v29, v9\n\t" \
"v_fma_f32 v10, -v40, v30, v10\n\t" \
"v_fma_f32 v11, -v40, v31, v11\n\t" \
"v_fma_f32 v12, -v42, v28, v12\n\t" \
"v_fma_f32 v13, -v42, v29, v13\n\t" \
"v_fma_f32 v14, -v42, v30, v14\n\t" \
"v_fma_f32 v15, -v42, v31, v15\n\t" \
"v_mul_f32 v8, v41, v8\n\t" \
"v_mul_f32 v9, v41, v9\n\t" \
"v_mul_f32 v10, v41, v10\n\t" \
"v_mul_f32 v11, v41, v11\n\t" \
"v_mul_f32 v12, v43, v12\n\t" \
"v_mul_f32 v13, v43, v13\n\t" \
"v_mul_f32 v14, v43, v14\n\t" \
"v_mul_f32 v15, v43, v15\n\t" \
"v_mul_f32 v46, v8, v8\n\t" \
"v_mul_f32 v47, v12, v12\n\t" \
"v_fmac_f32 v46, v9, v9\n\t" \
"v_fmac_f32 v47, v13, v13\n\t" \
"v_fmac_f32 v46, v10, v10\n\t" \
"v_fmac_f32 v47, v14, v14\n\t" \
"v_fmac_f32 v46, v11, v11\n\t" \
"v_fmac_f32 v47, v15, v15\n\t" \
"v_cvt_pk_bf16_f32 v8, v8, v9\n\t" \
"v_cvt_pk_bf16_f32 v9, v10, v11\n\t" \
"v_cvt_pk_bf16_f32 v10, v12, v13\n\t" \
"v_cvt_pk_bf16_f32 v11, v14, v15\n\t" \
"v_fma_f32 v0, -v40, v24, v0\n\t" \
"v_fma_f32 v1, -v40, v25, v1\n\t" \
"v_permlane32_swap_b32 v8, v10\n\t" \
"v_permlane32_swap_b32 v9, v11\n\t" \
"s_add_u32 s88, %[tb], 0x300000\n\t" \
"buffer_store_dwordx4 v[8:11], v162, %[rs], s88 offen sc1\n\t" \
"v_permlane32_swap_b32 v46, v47\n\t" \
"v_fma_f32 v2, -v40, v26, v2\n\t" \
"v_fma_f32 v3, -v40, v27, v3\n\t" \
"v_fma_f32 v4, -v42, v24, v4\n\t" \
"v_fma_f32 v5, -v42, v25, v5\n\t" \
"v_fma_f32 v6, -v42, v26, v6\n\t" \
"v_fma_f32 v7, -v42, v27, v7\n\t" \
"v_add_f32 v46, v46, v47\n\t" \
"v_mul_f32 v0, v44, v0\n\t" \
"v_mul_f32 v1, v44, v1\n\t" \
"v_mul_f32 v2, v44, v2\n\t" \
"v_mul_f32 v3, v44, v3\n\t" \
"v_mov_b32 v47, v46\n\t" \
"v_mul_f32 v4, v45, v4\n\t" \
"v_mul_f32 v5, v45, v5\n\t" \
"v_mul_f32 v6, v45, v6\n\t" \
"v_mul_f32 v7, v45, v7\n\t" \
"v_permlane16_swap_b32 v46, v47\n\t" \
"v_cvt_pk_bf16_f32 v0, v0, v1\n\t" \
"v_cvt_pk_bf16_f32 v1, v2, v3\n\t" \
"v_add_f32 v46, v46, v47\n\t" \
"v_cvt_pk_bf16_f32 v2, v4, v5\n\t" \
"v_cvt_pk_bf16_f32 v3, v6, v7\n\t" \
"s_waitcnt lgkmcnt(0)\n\t" \
"v_max_f32_dpp v46, v46, v46 quad_perm:[1,0,3,2] row_mask:0xf bank_mask:0xf\n\t" \
"v_permlane32_swap_b32 v0, v2\n\t" \
"v_permlane32_swap_b32 v1, v3\n\t" \
"s_add_u32 s88, %[tb], 0x100000\n\t" \
"buffer_store_dwordx4 v[0:3], v160, %[rs], s88 offen sc1\n\t" \
"v_fma_f32 v16, -v60, v32, v16\n\t" \
"v_fma_f32 v17, -v62, v32, v17\n\t" \
"v_max_f32_dpp v46, v46, v46 quad_perm:[2,3,0,1] row_mask:0xf bank_mask:0xf\n\t" \
"v_fma_f32 v18, -v64, v32, v18\n\t" \
"v_fma_f32 v19, -v66, v32, v19\n\t" \
"v_fma_f32 v20, -v68, v32, v20\n\t" \
"v_fma_f32 v21, -v70, v32, v21\n\t" \
"v_max_f32_dpp v46, v46, v46 row_half_mirror row_mask:0xf bank_mask:0xf\n\t" \
"v_fma_f32 v22, -v72, v32, v22\n\t" \
"v_fma_f32 v23, -v74, v32, v23\n\t" \
"v_mul_f32 v16, v61, v16\n\t" \
"v_mul_f32 v17, v63, v17\n\t" \
"v_max_f32_dpp v46, v46, v46 row_mirror row_mask:0xf bank_mask:0xf\n\t" \
"v_mul_f32 v18, v65, v18\n\t" \
"v_mul_f32 v19, v67, v19\n\t" \
"v_mul_f32 v20, v69, v20\n\t" \
"v_mul_f32 v21, v71, v21\n\t" \
"v_mul_f32 v22, v73, v22\n\t" \
"v_mul_f32 v23, v75, v23\n\t" \
"v_cvt_pk_bf16_f32 v16, v16, v17\n\t" \
"v_cvt_pk_bf16_f32 v17, v18, v19\n\t" \
"v_cvt_pk_bf16_f32 v18, v20, v21\n\t" \
"v_cvt_pk_bf16_f32 v19, v22, v23\n\t" \
"s_nop 1\n\t" \
"v_permlane32_swap_b32 v16, v18\n\t" \
"v_permlane32_swap_b32 v17, v19\n\t" \
"s_add_u32 s88, %[tb], 0x500000\n\t" \
"buffer_store_dwordx4 v[16:19], v161, %[rs], s88 offen sc1\n\t" \
"s_mov_b32 exec_lo, 0x1\n\t" \
"s_mov_b32 exec_hi, 0x1\n\t" \
"buffer_store_dword v46, v163, %[rs], %[ko] offen sc1\n\t" \
"s_mov_b64 exec, -1\n\t" \
"s_waitcnt vmcnt(0)\n\t" \
"s_barrier\n\t"

#define P1_LOOP \
"ds_read_b64_tr_b16 v[56:57], v90 offset:1024\n\t" \
"ds_read_b64_tr_b16 v[58:59], v90 offset:1536\n\t" \
"ds_read_b128 v[40:43], v88\n\t" \
"ds_read_b128 v[44:47], v88 offset:8192\n\t" \
"ds_read_b64_tr_b16 v[60:61], v90 offset:2048\n\t" \
"ds_read_b64_tr_b16 v[62:63], v90 offset:2560\n\t" \
"ds_read_b64_tr_b16 v[64:65], v90 offset:3072\n\t" \
"ds_read_b64_tr_b16 v[66:67], v90 offset:3584\n\t" \
"s_waitcnt lgkmcnt(7)\n\t" \
"ds_read_b64_tr_b16 v[68:69], v90 offset:17408\n\t" \
"ds_read_b64_tr_b16 v[70:71], v90 offset:17920\n\t" \
"ds_read_b128 v[48:51], v89\n\t" \
"ds_read_b128 v[52:55], v89 offset:8192\n\t" \
"ds_read_b64_tr_b16 v[72:73], v90 offset:18432\n\t" \
"ds_read_b64_tr_b16 v[74:75], v90 offset:18944\n\t" \
"ds_read_b64_tr_b16 v[76:77], v90 offset:19456\n\t" \
"ds_read_b64_tr_b16 v[78:79], v90 offset:19968\n\t" \
"v_add_u32 v88, 0x8000, v88\n\t" \
"v_add_u32 v89, 0x8000, v89\n\t" \
"v_add_u32 v90, 0x8000, v90\n\t" \
"s_add_u32 m0, %[lw], 0x10000\n\t" \
"s_nop 0\n\t" \
"buffer_load_dwordx4 v92, s[80:83], s89 offen offset:0 lds\n\t" \
"buffer_load_dwordx4 v93, s[84:87], s90 offen offset:1024 lds\n\t" \
"buffer_load_dwordx4 v93, s[84:87], s90 offen offset:2048 lds\n\t" \
"buffer_load_dwordx4 v93, s[84:87], s90 offen offset:3072 lds\n\t" \
"s_add_u32 s88, s88, 1\n\t" \
"s_and_b32 s88, s88, 7\n\t" \
"s_lshl_b32 s89, s88, 7\n\t" \
"s_mul_i32 s90, s88, 0x30000\n\t" \
"s_add_u32 m0, %[lw], 0x18000\n\t" \
"s_waitcnt vmcnt(6) lgkmcnt(8)\n\t" \
"s_barrier\n\t" \
"v_mfma_f32_16x16x32_bf16 v[0:3], v[56:59], v[40:43], 0\n\t" \
"ds_read_b64_tr_b16 v[128:129], v90 offset:1024\n\t" \
"ds_read_b64_tr_b16 v[130:131], v90 offset:1536\n\t" \
"v_mfma_f32_16x16x32_bf16 v[4:7], v[56:59], v[44:47], 0\n\t" \
"ds_read_b128 v[112:115], v88\n\t" \
"ds_read_b128 v[116:119], v88 offset:8192\n\t" \
"buffer_load_dwordx4 v92, s[80:83], s89 offen offset:0 lds\n\t" \
"v_mfma_f32_16x16x32_bf16 v[8:11], v[60:63], v[40:43], 0\n\t" \
"ds_read_b64_tr_b16 v[132:133], v90 offset:2048\n\t" \
"ds_read_b64_tr_b16 v[134:135], v90 offset:2560\n\t" \
"v_mfma_f32_16x16x32_bf16 v[12:15], v[60:63], v[44:47], 0\n\t" \
"s_waitcnt lgkmcnt(6)\n\t" \
"ds_read_b64_tr_b16 v[136:137], v90 offset:3072\n\t" \
"ds_read_b64_tr_b16 v[138:139], v90 offset:3584\n\t" \
"buffer_load_dwordx4 v93, s[84:87], s90 offen offset:1024 lds\n\t" \
"v_mfma_f32_16x16x32_bf16 v[16:19], v[40:43], v[64:67], 0\n\t" \
"v_mfma_f32_16x16x32_bf16 v[20:23], v[44:47], v[64:67], 0\n\t" \
"s_cmp_lg_u32 %[m], 0\n\t" \
"s_cbranch_scc1 1f\n\t" \
"v_mfma_f32_16x16x32_bf16 v[24:27], v[56:59], v[36:39], 0\n\t" \
"v_mfma_f32_16x16x32_bf16 v[28:31], v[60:63], v[36:39], 0\n\t" \
"v_mfma_f32_16x16x32_bf16 v[32:35], v[36:39], v[64:67], 0\n\t" \
"1:\n\t" \
"s_cmp_lg_u32 %[hl], 0\n\t" \
"s_cbranch_scc1 2f\n\t" \
"v_mfma_f32_16x16x32_bf16 v[80:83], v[36:39], v[40:43], 0\n\t" \
"v_mfma_f32_16x16x32_bf16 v[84:87], v[36:39], v[44:47], 0\n\t" \
"v_mfma_f32_16x16x32_bf16 v[96:99], v[40:43], v[40:43], 0\n\t" \
"v_mfma_f32_16x16x32_bf16 v[100:103], v[44:47], v[44:47], 0\n\t" \
"2:\n\t" \
"v_mfma_f32_16x16x32_bf16 v[0:3], v[68:71], v[48:51], v[0:3]\n\t" \
"buffer_load_dwordx4 v93, s[84:87], s90 offen offset:2048 lds\n\t" \
"ds_read_b64_tr_b16 v[140:141], v90 offset:17408\n\t" \
"ds_read_b64_tr_b16 v[142:143], v90 offset:17920\n\t" \
"v_mfma_f32_16x16x32_bf16 v[4:7], v[68:71], v[52:55], v[4:7]\n\t" \
"ds_read_b128 v[120:123], v89\n\t" \
"ds_read_b128 v[124:127], v89 offset:8192\n\t" \
"buffer_load_dwordx4 v93, s[84:87], s90 offen offset:3072 lds\n\t" \
"v_mfma_f32_16x16x32_bf16 v[8:11], v[72:75], v[48:51], v[8:11]\n\t" \
"ds_read_b64_tr_b16 v[144:145], v90 offset:18432\n\t" \
"ds_read_b64_tr_b16 v[146:147], v90 offset:18944\n\t" \
"v_mfma_f32_16x16x32_bf16 v[12:15], v[72:75], v[52:55], v[12:15]\n\t" \
"s_waitcnt lgkmcnt(13)\n\t" \
"ds_read_b64_tr_b16 v[148:149], v90 offset:19456\n\t" \
"ds_read_b64_tr_b16 v[150:151], v90 offset:19968\n\t" \
"v_mfma_f32_16x16x32_bf16 v[16:19], v[48:51], v[76:79], v[16:19]\n\t" \
"s_add_u32 s88, s88, 1\n\t" \
"s_and_b32 s88, s88, 7\n\t" \
"s_lshl_b32 s89, s88, 7\n\t" \
"s_mul_i32 s90, s88, 0x30000\n\t" \
"s_add_u32 m0, %[lw], 0x20000\n\t" \
"v_add_u32 v88, 0x8000, v88\n\t" \
"v_add_u32 v89, 0x8000, v89\n\t" \
"v_add_u32 v90, 0x8000, v90\n\t" \
"v_mfma_f32_16x16x32_bf16 v[20:23], v[52:55], v[76:79], v[20:23]\n\t" \
"s_cmp_lg_u32 %[m], 1\n\t" \
"s_cbranch_scc1 3f\n\t" \
"v_mfma_f32_16x16x32_bf16 v[24:27], v[68:71], v[36:39], 0\n\t" \
"v_mfma_f32_16x16x32_bf16 v[28:31], v[72:75], v[36:39], 0\n\t" \
"v_mfma_f32_16x16x32_bf16 v[32:35], v[36:39], v[76:79], 0\n\t" \
"3:\n\t" \
"s_cmp_lg_u32 %[hl], 1\n\t" \
"s_cbranch_scc1 4f\n\t" \
"v_mfma_f32_16x16x32_bf16 v[80:83], v[36:39], v[48:51], 0\n\t" \
"v_mfma_f32_16x16x32_bf16 v[84:87], v[36:39], v[52:55], 0\n\t" \
"v_mfma_f32_16x16x32_bf16 v[96:99], v[48:51], v[48:51], 0\n\t" \
"v_mfma_f32_16x16x32_bf16 v[100:103], v[52:55], v[52:55], 0\n\t" \
"4:\n\t" \
"s_waitcnt vmcnt(4) lgkmcnt(8)\n\t" \
"s_barrier\n\t" \
"v_mfma_f32_16x16x32_bf16 v[0:3], v[128:131], v[112:115], v[0:3]\n\t" \
"ds_read_b64_tr_b16 v[56:57], v90 offset:1024\n\t" \
"ds_read_b64_tr_b16 v[58:59], v90 offset:1536\n\t" \
"v_mfma_f32_16x16x32_bf16 v[4:7], v[128:131], v[116:119], v[4:7]\n\t" \
"ds_read_b128 v[40:43], v88\n\t" \
"ds_read_b128 v[44:47], v88 offset:8192\n\t" \
"buffer_load_dwordx4 v92, s[80:83], s89 offen offset:0 lds\n\t" \
"v_mfma_f32_16x16x32_bf16 v[8:11], v[132:135], v[112:115], v[8:11]\n\t" \
"ds_read_b64_tr_b16 v[60:61], v90 offset:2048\n\t" \
"ds_read_b64_tr_b16 v[62:63], v90 offset:2560\n\t" \
"v_mfma_f32_16x16x32_bf16 v[12:15], v[132:135], v[116:119], v[12:15]\n\t" \
"s_waitcnt lgkmcnt(6)\n\t" \
"ds_read_b64_tr_b16 v[64:65], v90 offset:3072\n\t" \
"ds_read_b64_tr_b16 v[66:67], v90 offset:3584\n\t" \
"buffer_load_dwordx4 v93, s[84:87], s90 offen offset:1024 lds\n\t" \
"v_mfma_f32_16x16x32_bf16 v[16:19], v[112:115], v[136:139], v[16:19]\n\t" \
"v_mfma_f32_16x16x32_bf16 v[20:23], v[116:119], v[136:139], v[20:23]\n\t" \
"s_cmp_lg_u32 %[m], 0\n\t" \
"s_cbranch_scc1 5f\n\t" \
"v_mfma_f32_16x16x32_bf16 v[24:27], v[128:131], v[36:39], v[24:27]\n\t" \
"v_mfma_f32_16x16x32_bf16 v[28:31], v[132:135], v[36:39], v[28:31]\n\t" \
"v_mfma_f32_16x16x32_bf16 v[32:35], v[36:39], v[136:139], v[32:35]\n\t" \
"5:\n\t" \
"s_cmp_lg_u32 %[hl], 2\n\t" \
"s_cbranch_scc1 6f\n\t" \
"v_mfma_f32_16x16x32_bf16 v[80:83], v[36:39], v[112:115], 0\n\t" \
"v_mfma_f32_16x16x32_bf16 v[84:87], v[36:39], v[116:119], 0\n\t" \
"v_mfma_f32_16x16x32_bf16 v[96:99], v[112:115], v[112:115], 0\n\t" \
"v_mfma_f32_16x16x32_bf16 v[100:103], v[116:119], v[116:119], 0\n\t" \
"6:\n\t" \
"v_mfma_f32_16x16x32_bf16 v[0:3], v[140:143], v[120:123], v[0:3]\n\t" \
"buffer_load_dwordx4 v93, s[84:87], s90 offen offset:2048 lds\n\t" \
"ds_read_b64_tr_b16 v[68:69], v90 offset:17408\n\t" \
"ds_read_b64_tr_b16 v[70:71], v90 offset:17920\n\t" \
"v_mfma_f32_16x16x32_bf16 v[4:7], v[140:143], v[124:127], v[4:7]\n\t" \
"ds_read_b128 v[48:51], v89\n\t" \
"ds_read_b128 v[52:55], v89 offset:8192\n\t" \
"buffer_load_dwordx4 v93, s[84:87], s90 offen offset:3072 lds\n\t" \
"v_mfma_f32_16x16x32_bf16 v[8:11], v[144:147], v[120:123], v[8:11]\n\t" \
"ds_read_b64_tr_b16 v[72:73], v90 offset:18432\n\t" \
"ds_read_b64_tr_b16 v[74:75], v90 offset:18944\n\t" \
"v_mfma_f32_16x16x32_bf16 v[12:15], v[144:147], v[124:127], v[12:15]\n\t" \
"s_waitcnt lgkmcnt(13)\n\t" \
"ds_read_b64_tr_b16 v[76:77], v90 offset:19456\n\t" \
"ds_read_b64_tr_b16 v[78:79], v90 offset:19968\n\t" \
"v_mfma_f32_16x16x32_bf16 v[16:19], v[120:123], v[148:151], v[16:19]\n\t" \
"s_add_u32 s88, s88, 1\n\t" \
"s_and_b32 s88, s88, 7\n\t" \
"s_lshl_b32 s89, s88, 7\n\t" \
"s_mul_i32 s90, s88, 0x30000\n\t" \
"s_add_u32 m0, %[lw], 0x0\n\t" \
"v_add_u32 v88, 0x8000, v88\n\t" \
"v_add_u32 v89, 0x8000, v89\n\t" \
"v_add_u32 v90, 0x8000, v90\n\t" \
"v_mfma_f32_16x16x32_bf16 v[20:23], v[124:127], v[148:151], v[20:23]\n\t" \
"s_cmp_lg_u32 %[m], 1\n\t" \
"s_cbranch_scc1 7f\n\t" \
"v_mfma_f32_16x16x32_bf16 v[24:27], v[140:143], v[36:39], v[24:27]\n\t" \
"v_mfma_f32_16x16x32_bf16 v[28:31], v[144:147], v[36:39], v[28:31]\n\t" \
"v_mfma_f32_16x16x32_bf16 v[32:35], v[36:39], v[148:151], v[32:35]\n\t" \
"7:\n\t" \
"s_cmp_lg_u32 %[hl], 3\n\t" \
"s_cbranch_scc1 8f\n\t" \
"v_mfma_f32_16x16x32_bf16 v[80:83], v[36:39], v[120:123], 0\n\t" \
"v_mfma_f32_16x16x32_bf16 v[84:87], v[36:39], v[124:127], 0\n\t" \
"v_mfma_f32_16x16x32_bf16 v[96:99], v[120:123], v[120:123], 0\n\t" \
"v_mfma_f32_16x16x32_bf16 v[100:103], v[124:127], v[124:127], 0\n\t" \
"8:\n\t" \
"s_waitcnt vmcnt(4) lgkmcnt(8)\n\t" \
"s_barrier\n\t" \
"v_mfma_f32_16x16x32_bf16 v[0:3], v[56:59], v[40:43], v[0:3]\n\t" \
"ds_read_b64_tr_b16 v[128:129], v90 offset:1024\n\t" \
"ds_read_b64_tr_b16 v[130:131], v90 offset:1536\n\t" \
"v_mfma_f32_16x16x32_bf16 v[4:7], v[56:59], v[44:47], v[4:7]\n\t" \
"ds_read_b128 v[112:115], v88\n\t" \
"ds_read_b128 v[116:119], v88 offset:8192\n\t" \
"buffer_load_dwordx4 v92, s[80:83], s89 offen offset:0 lds\n\t" \
"v_mfma_f32_16x16x32_bf16 v[8:11], v[60:63], v[40:43], v[8:11]\n\t" \
"ds_read_b64_tr_b16 v[132:133], v90 offset:2048\n\t" \
"ds_read_b64_tr_b16 v[134:135], v90 offset:2560\n\t" \
"v_mfma_f32_16x16x32_bf16 v[12:15], v[60:63], v[44:47], v[12:15]\n\t" \
"s_waitcnt lgkmcnt(6)\n\t" \
"ds_read_b64_tr_b16 v[136:137], v90 offset:3072\n\t" \
"ds_read_b64_tr_b16 v[138:139], v90 offset:3584\n\t" \
"buffer_load_dwordx4 v93, s[84:87], s90 offen offset:1024 lds\n\t" \
"v_mfma_f32_16x16x32_bf16 v[16:19], v[40:43], v[64:67], v[16:19]\n\t" \
"v_mfma_f32_16x16x32_bf16 v[20:23], v[44:47], v[64:67], v[20:23]\n\t" \
"s_cmp_lg_u32 %[m], 0\n\t" \
"s_cbranch_scc1 9f\n\t" \
"v_mfma_f32_16x16x32_bf16 v[24:27], v[56:59], v[36:39], v[24:27]\n\t" \
"v_mfma_f32_16x16x32_bf16 v[28:31], v[60:63], v[36:39], v[28:31]\n\t" \
"v_mfma_f32_16x16x32_bf16 v[32:35], v[36:39], v[64:67], v[32:35]\n\t" \
"9:\n\t" \
"s_cmp_lg_u32 %[hl], 0\n\t" \
"s_cbranch_scc1 10f\n\t" \
"v_mfma_f32_16x16x32_bf16 v[80:83], v[36:39], v[40:43], v[80:83]\n\t" \
"v_mfma_f32_16x16x32_bf16 v[84:87], v[36:39], v[44:47], v[84:87]\n\t" \
"v_mfma_f32_16x16x32_bf16 v[96:99], v[40:43], v[40:43], v[96:99]\n\t" \
"v_mfma_f32_16x16x32_bf16 v[100:103], v[44:47], v[44:47], v[100:103]\n\t" \
"10:\n\t" \
"v_mfma_f32_16x16x32_bf16 v[0:3], v[68:71], v[48:51], v[0:3]\n\t" \
"buffer_load_dwordx4 v93, s[84:87], s90 offen offset:2048 lds\n\t" \
"ds_read_b64_tr_b16 v[140:141], v90 offset:17408\n\t" \
"ds_read_b64_tr_b16 v[142:143], v90 offset:17920\n\t" \
"v_mfma_f32_16x16x32_bf16 v[4:7], v[68:71], v[52:55], v[4:7]\n\t" \
"ds_read_b128 v[120:123], v89\n\t" \
"ds_read_b128 v[124:127], v89 offset:8192\n\t" \
"buffer_load_dwordx4 v93, s[84:87], s90 offen offset:3072 lds\n\t" \
"v_mfma_f32_16x16x32_bf16 v[8:11], v[72:75], v[48:51], v[8:11]\n\t" \
"ds_read_b64_tr_b16 v[144:145], v90 offset:18432\n\t" \
"ds_read_b64_tr_b16 v[146:147], v90 offset:18944\n\t" \
"v_mfma_f32_16x16x32_bf16 v[12:15], v[72:75], v[52:55], v[12:15]\n\t" \
"s_waitcnt lgkmcnt(13)\n\t" \
"ds_read_b64_tr_b16 v[148:149], v90 offset:19456\n\t" \
"ds_read_b64_tr_b16 v[150:151], v90 offset:19968\n\t" \
"v_mfma_f32_16x16x32_bf16 v[16:19], v[48:51], v[76:79], v[16:19]\n\t" \
"s_add_u32 s88, s88, 1\n\t" \
"s_and_b32 s88, s88, 7\n\t" \
"s_lshl_b32 s89, s88, 7\n\t" \
"s_mul_i32 s90, s88, 0x30000\n\t" \
"s_add_u32 m0, %[lw], 0x8000\n\t" \
"v_add_u32 v88, 0x8000, v88\n\t" \
"v_add_u32 v89, 0x8000, v89\n\t" \
"v_add_u32 v90, 0x8000, v90\n\t" \
"v_mfma_f32_16x16x32_bf16 v[20:23], v[52:55], v[76:79], v[20:23]\n\t" \
"s_cmp_lg_u32 %[m], 1\n\t" \
"s_cbranch_scc1 11f\n\t" \
"v_mfma_f32_16x16x32_bf16 v[24:27], v[68:71], v[36:39], v[24:27]\n\t" \
"v_mfma_f32_16x16x32_bf16 v[28:31], v[72:75], v[36:39], v[28:31]\n\t" \
"v_mfma_f32_16x16x32_bf16 v[32:35], v[36:39], v[76:79], v[32:35]\n\t" \
"11:\n\t" \
"s_cmp_lg_u32 %[hl], 1\n\t" \
"s_cbranch_scc1 12f\n\t" \
"v_mfma_f32_16x16x32_bf16 v[80:83], v[36:39], v[48:51], v[80:83]\n\t" \
"v_mfma_f32_16x16x32_bf16 v[84:87], v[36:39], v[52:55], v[84:87]\n\t" \
"v_mfma_f32_16x16x32_bf16 v[96:99], v[48:51], v[48:51], v[96:99]\n\t" \
"v_mfma_f32_16x16x32_bf16 v[100:103], v[52:55], v[52:55], v[100:103]\n\t" \
"12:\n\t" \
"s_waitcnt vmcnt(4) lgkmcnt(8)\n\t" \
"s_barrier\n\t" \
"v_mfma_f32_16x16x32_bf16 v[0:3], v[128:131], v[112:115], v[0:3]\n\t" \
"ds_read_b64_tr_b16 v[56:57], v90 offset:1024\n\t" \
"ds_read_b64_tr_b16 v[58:59], v90 offset:1536\n\t" \
"v_mfma_f32_16x16x32_bf16 v[4:7], v[128:131], v[116:119], v[4:7]\n\t" \
"ds_read_b128 v[40:43], v88\n\t" \
"ds_read_b128 v[44:47], v88 offset:8192\n\t" \
"buffer_load_dwordx4 v92, s[80:83], s89 offen offset:0 lds\n\t" \
"v_mfma_f32_16x16x32_bf16 v[8:11], v[132:135], v[112:115], v[8:11]\n\t" \
"ds_read_b64_tr_b16 v[60:61], v90 offset:2048\n\t" \
"ds_read_b64_tr_b16 v[62:63], v90 offset:2560\n\t" \
"v_mfma_f32_16x16x32_bf16 v[12:15], v[132:135], v[116:119], v[12:15]\n\t" \
"s_waitcnt lgkmcnt(6)\n\t" \
"ds_read_b64_tr_b16 v[64:65], v90 offset:3072\n\t" \
"ds_read_b64_tr_b16 v[66:67], v90 offset:3584\n\t" \
"buffer_load_dwordx4 v93, s[84:87], s90 offen offset:1024 lds\n\t" \
"v_mfma_f32_16x16x32_bf16 v[16:19], v[112:115], v[136:139], v[16:19]\n\t" \
"v_mfma_f32_16x16x32_bf16 v[20:23], v[116:119], v[136:139], v[20:23]\n\t" \
"s_cmp_lg_u32 %[m], 0\n\t" \
"s_cbranch_scc1 13f\n\t" \
"v_mfma_f32_16x16x32_bf16 v[24:27], v[128:131], v[36:39], v[24:27]\n\t" \
"v_mfma_f32_16x16x32_bf16 v[28:31], v[132:135], v[36:39], v[28:31]\n\t" \
"v_mfma_f32_16x16x32_bf16 v[32:35], v[36:39], v[136:139], v[32:35]\n\t" \
"13:\n\t" \
"s_cmp_lg_u32 %[hl], 2\n\t" \
"s_cbranch_scc1 14f\n\t" \
"v_mfma_f32_16x16x32_bf16 v[80:83], v[36:39], v[112:115], v[80:83]\n\t" \
"v_mfma_f32_16x16x32_bf16 v[84:87], v[36:39], v[116:119], v[84:87]\n\t" \
"v_mfma_f32_16x16x32_bf16 v[96:99], v[112:115], v[112:115], v[96:99]\n\t" \
"v_mfma_f32_16x16x32_bf16 v[100:103], v[116:119], v[116:119], v[100:103]\n\t" \
"14:\n\t" \
"v_mfma_f32_16x16x32_bf16 v[0:3], v[140:143], v[120:123], v[0:3]\n\t" \
"buffer_load_dwordx4 v93, s[84:87], s90 offen offset:2048 lds\n\t" \
"ds_read_b64_tr_b16 v[68:69], v90 offset:17408\n\t" \
"ds_read_b64_tr_b16 v[70:71], v90 offset:17920\n\t" \
"v_mfma_f32_16x16x32_bf16 v[4:7], v[140:143], v[124:127], v[4:7]\n\t" \
"ds_read_b128 v[48:51], v89\n\t" \
"ds_read_b128 v[52:55], v89 offset:8192\n\t" \
"buffer_load_dwordx4 v93, s[84:87], s90 offen offset:3072 lds\n\t" \
"v_mfma_f32_16x16x32_bf16 v[8:11], v[144:147], v[120:123], v[8:11]\n\t" \
"ds_read_b64_tr_b16 v[72:73], v90 offset:18432\n\t" \
"ds_read_b64_tr_b16 v[74:75], v90 offset:18944\n\t" \
"v_mfma_f32_16x16x32_bf16 v[12:15], v[144:147], v[124:127], v[12:15]\n\t" \
"s_waitcnt lgkmcnt(13)\n\t" \
"ds_read_b64_tr_b16 v[76:77], v90 offset:19456\n\t" \
"ds_read_b64_tr_b16 v[78:79], v90 offset:19968\n\t" \
"v_mfma_f32_16x16x32_bf16 v[16:19], v[120:123], v[148:151], v[16:19]\n\t" \
"s_add_u32 s88, s88, 1\n\t" \
"s_and_b32 s88, s88, 7\n\t" \
"s_lshl_b32 s89, s88, 7\n\t" \
"s_mul_i32 s90, s88, 0x30000\n\t" \
"global_load_dword v164, v165, %[wo2]\n\t" \
"s_add_u32 m0, %[lw], 0x10000\n\t" \
"v_add_u32 v88, 0xfffe0000, v88\n\t" \
"v_add_u32 v89, 0xfffe0000, v89\n\t" \
"v_add_u32 v90, 0xfffe0000, v90\n\t" \
"v_mfma_f32_16x16x32_bf16 v[20:23], v[124:127], v[148:151], v[20:23]\n\t" \
"s_cmp_lg_u32 %[m], 1\n\t" \
"s_cbranch_scc1 15f\n\t" \
"v_mfma_f32_16x16x32_bf16 v[24:27], v[140:143], v[36:39], v[24:27]\n\t" \
"v_mfma_f32_16x16x32_bf16 v[28:31], v[144:147], v[36:39], v[28:31]\n\t" \
"v_mfma_f32_16x16x32_bf16 v[32:35], v[36:39], v[148:151], v[32:35]\n\t" \
"15:\n\t" \
"s_cmp_lg_u32 %[hl], 3\n\t" \
"s_cbranch_scc1 16f\n\t" \
"v_mfma_f32_16x16x32_bf16 v[80:83], v[36:39], v[120:123], v[80:83]\n\t" \
"v_mfma_f32_16x16x32_bf16 v[84:87], v[36:39], v[124:127], v[84:87]\n\t" \
"v_mfma_f32_16x16x32_bf16 v[96:99], v[120:123], v[120:123], v[96:99]\n\t" \
"v_mfma_f32_16x16x32_bf16 v[100:103], v[124:127], v[124:127], v[100:103]\n\t" \
"16:\n\t" \
"s_waitcnt vmcnt(5) lgkmcnt(8)\n\t" \
"s_barrier\n\t" \
"v_mfma_f32_16x16x32_bf16 v[0:3], v[56:59], v[40:43], v[0:3]\n\t" \
"ds_read_b64_tr_b16 v[128:129], v90 offset:1024\n\t" \
"ds_read_b64_tr_b16 v[130:131], v90 offset:1536\n\t" \
"v_mfma_f32_16x16x32_bf16 v[4:7], v[56:59], v[44:47], v[4:7]\n\t" \
"ds_read_b128 v[112:115], v88\n\t" \
"ds_read_b128 v[116:119], v88 offset:8192\n\t" \
"buffer_load_dwordx4 v92, s[80:83], s89 offen offset:0 lds\n\t" \
"v_mfma_f32_16x16x32_bf16 v[8:11], v[60:63], v[40:43], v[8:11]\n\t" \
"ds_read_b64_tr_b16 v[132:133], v90 offset:2048\n\t" \
"ds_read_b64_tr_b16 v[134:135], v90 offset:2560\n\t" \
"v_mfma_f32_16x16x32_bf16 v[12:15], v[60:63], v[44:47], v[12:15]\n\t" \
"s_waitcnt lgkmcnt(6)\n\t" \
"ds_read_b64_tr_b16 v[136:137], v90 offset:3072\n\t" \
"ds_read_b64_tr_b16 v[138:139], v90 offset:3584\n\t" \
"buffer_load_dwordx4 v93, s[84:87], s90 offen offset:1024 lds\n\t" \
"v_mfma_f32_16x16x32_bf16 v[16:19], v[40:43], v[64:67], v[16:19]\n\t" \
"v_mfma_f32_16x16x32_bf16 v[20:23], v[44:47], v[64:67], v[20:23]\n\t" \
"s_cmp_lg_u32 %[m], 0\n\t" \
"s_cbranch_scc1 17f\n\t" \
"v_mfma_f32_16x16x32_bf16 v[24:27], v[56:59], v[36:39], v[24:27]\n\t" \
"v_mfma_f32_16x16x32_bf16 v[28:31], v[60:63], v[36:39], v[28:31]\n\t" \
"v_mfma_f32_16x16x32_bf16 v[32:35], v[36:39], v[64:67], v[32:35]\n\t" \
"17:\n\t" \
"s_cmp_lg_u32 %[hl], 0\n\t" \
"s_cbranch_scc1 18f\n\t" \
"v_mfma_f32_16x16x32_bf16 v[80:83], v[36:39], v[40:43], v[80:83]\n\t" \
"v_mfma_f32_16x16x32_bf16 v[84:87], v[36:39], v[44:47], v[84:87]\n\t" \
"v_mfma_f32_16x16x32_bf16 v[96:99], v[40:43], v[40:43], v[96:99]\n\t" \
"v_mfma_f32_16x16x32_bf16 v[100:103], v[44:47], v[44:47], v[100:103]\n\t" \
"18:\n\t" \
"v_mfma_f32_16x16x32_bf16 v[0:3], v[68:71], v[48:51], v[0:3]\n\t" \
"buffer_load_dwordx4 v93, s[84:87], s90 offen offset:2048 lds\n\t" \
"ds_read_b64_tr_b16 v[140:141], v90 offset:17408\n\t" \
"ds_read_b64_tr_b16 v[142:143], v90 offset:17920\n\t" \
"v_mfma_f32_16x16x32_bf16 v[4:7], v[68:71], v[52:55], v[4:7]\n\t" \
"ds_read_b128 v[120:123], v89\n\t" \
"ds_read_b128 v[124:127], v89 offset:8192\n\t" \
"buffer_load_dwordx4 v93, s[84:87], s90 offen offset:3072 lds\n\t" \
"v_mfma_f32_16x16x32_bf16 v[8:11], v[72:75], v[48:51], v[8:11]\n\t" \
"ds_read_b64_tr_b16 v[144:145], v90 offset:18432\n\t" \
"ds_read_b64_tr_b16 v[146:147], v90 offset:18944\n\t" \
"v_mfma_f32_16x16x32_bf16 v[12:15], v[72:75], v[52:55], v[12:15]\n\t" \
"s_waitcnt lgkmcnt(13)\n\t" \
"ds_read_b64_tr_b16 v[148:149], v90 offset:19456\n\t" \
"ds_read_b64_tr_b16 v[150:151], v90 offset:19968\n\t" \
"v_mfma_f32_16x16x32_bf16 v[16:19], v[48:51], v[76:79], v[16:19]\n\t" \
"s_add_u32 m0, %[lw], 0x18000\n\t" \
"v_add_u32 v88, 0x8000, v88\n\t" \
"v_add_u32 v89, 0x8000, v89\n\t" \
"v_add_u32 v90, 0x8000, v90\n\t" \
"v_mfma_f32_16x16x32_bf16 v[20:23], v[52:55], v[76:79], v[20:23]\n\t" \
"s_cmp_lg_u32 %[m], 1\n\t" \
"s_cbranch_scc1 19f\n\t" \
"v_mfma_f32_16x16x32_bf16 v[24:27], v[68:71], v[36:39], v[24:27]\n\t" \
"v_mfma_f32_16x16x32_bf16 v[28:31], v[72:75], v[36:39], v[28:31]\n\t" \
"v_mfma_f32_16x16x32_bf16 v[32:35], v[36:39], v[76:79], v[32:35]\n\t" \
"19:\n\t" \
"s_cmp_lg_u32 %[hl], 1\n\t" \
"s_cbranch_scc1 20f\n\t" \
"v_mfma_f32_16x16x32_bf16 v[80:83], v[36:39], v[48:51], v[80:83]\n\t" \
"v_mfma_f32_16x16x32_bf16 v[84:87], v[36:39], v[52:55], v[84:87]\n\t" \
"v_mfma_f32_16x16x32_bf16 v[96:99], v[48:51], v[48:51], v[96:99]\n\t" \
"v_mfma_f32_16x16x32_bf16 v[100:103], v[52:55], v[52:55], v[100:103]\n\t" \
"20:\n\t" \
"s_waitcnt vmcnt(4) lgkmcnt(8)\n\t" \
"s_barrier\n\t" \
"v_mfma_f32_16x16x32_bf16 v[0:3], v[128:131], v[112:115], v[0:3]\n\t" \
"ds_read_b64_tr_b16 v[56:57], v90 offset:1024\n\t" \
"ds_read_b64_tr_b16 v[58:59], v90 offset:1536\n\t" \
"v_mfma_f32_16x16x32_bf16 v[4:7], v[128:131], v[116:119], v[4:7]\n\t" \
"ds_read_b128 v[40:43], v88\n\t" \
"ds_read_b128 v[44:47], v88 offset:8192\n\t" \
"global_load_lds_dwordx4 v104, s[92:93] offset:0\n\t" \
"v_mfma_f32_16x16x32_bf16 v[8:11], v[132:135], v[112:115], v[8:11]\n\t" \
"ds_read_b64_tr_b16 v[60:61], v90 offset:2048\n\t" \
"ds_read_b64_tr_b16 v[62:63], v90 offset:2560\n\t" \
"v_mfma_f32_16x16x32_bf16 v[12:15], v[132:135], v[116:119], v[12:15]\n\t" \
"s_waitcnt lgkmcnt(6)\n\t" \
"ds_read_b64_tr_b16 v[64:65], v90 offset:3072\n\t" \
"ds_read_b64_tr_b16 v[66:67], v90 offset:3584\n\t" \
"global_load_lds_dwordx4 v105, s[92:93] offset:1024\n\t" \
"v_mfma_f32_16x16x32_bf16 v[16:19], v[112:115], v[136:139], v[16:19]\n\t" \
"v_mfma_f32_16x16x32_bf16 v[20:23], v[116:119], v[136:139], v[20:23]\n\t" \
"s_cmp_lg_u32 %[m], 0\n\t" \
"s_cbranch_scc1 21f\n\t" \
"v_mfma_f32_16x16x32_bf16 v[24:27], v[128:131], v[36:39], v[24:27]\n\t" \
"v_mfma_f32_16x16x32_bf16 v[28:31], v[132:135], v[36:39], v[28:31]\n\t" \
"v_mfma_f32_16x16x32_bf16 v[32:35], v[36:39], v[136:139], v[32:35]\n\t" \
"21:\n\t" \
"s_cmp_lg_u32 %[hl], 2\n\t" \
"s_cbranch_scc1 22f\n\t" \
"v_mfma_f32_16x16x32_bf16 v[80:83], v[36:39], v[112:115], v[80:83]\n\t" \
"v_mfma_f32_16x16x32_bf16 v[84:87], v[36:39], v[116:119], v[84:87]\n\t" \
"v_mfma_f32_16x16x32_bf16 v[96:99], v[112:115], v[112:115], v[96:99]\n\t" \
"v_mfma_f32_16x16x32_bf16 v[100:103], v[116:119], v[116:119], v[100:103]\n\t" \
"22:\n\t" \
"v_mfma_f32_16x16x32_bf16 v[0:3], v[140:143], v[120:123], v[0:3]\n\t" \
"global_load_lds_dwordx4 v106, s[92:93] offset:2048\n\t" \
"ds_read_b64_tr_b16 v[68:69], v90 offset:17408\n\t" \
"ds_read_b64_tr_b16 v[70:71], v90 offset:17920\n\t" \
"v_mfma_f32_16x16x32_bf16 v[4:7], v[140:143], v[124:127], v[4:7]\n\t" \
"ds_read_b128 v[48:51], v89\n\t" \
"ds_read_b128 v[52:55], v89 offset:8192\n\t" \
"global_load_lds_dwordx4 v107, s[92:93] offset:3072\n\t" \
"v_mfma_f32_16x16x32_bf16 v[8:11], v[144:147], v[120:123], v[8:11]\n\t" \
"ds_read_b64_tr_b16 v[72:73], v90 offset:18432\n\t" \
"ds_read_b64_tr_b16 v[74:75], v90 offset:18944\n\t" \
"v_mfma_f32_16x16x32_bf16 v[12:15], v[144:147], v[124:127], v[12:15]\n\t" \
"s_waitcnt lgkmcnt(13)\n\t" \
"ds_read_b64_tr_b16 v[76:77], v90 offset:19456\n\t" \
"ds_read_b64_tr_b16 v[78:79], v90 offset:19968\n\t" \
"v_mfma_f32_16x16x32_bf16 v[16:19], v[120:123], v[148:151], v[16:19]\n\t" \
"s_add_u32 s92, s92, 0x20000\n\t" \
"s_addc_u32 s93, s93, 0\n\t" \
"s_add_u32 m0, %[lw], 0x20000\n\t" \
"v_add_u32 v88, 0x8000, v88\n\t" \
"v_add_u32 v89, 0x8000, v89\n\t" \
"v_add_u32 v90, 0x8000, v90\n\t" \
"v_mfma_f32_16x16x32_bf16 v[20:23], v[124:127], v[148:151], v[20:23]\n\t" \
"s_cmp_lg_u32 %[m], 1\n\t" \
"s_cbranch_scc1 23f\n\t" \
"v_mfma_f32_16x16x32_bf16 v[24:27], v[140:143], v[36:39], v[24:27]\n\t" \
"v_mfma_f32_16x16x32_bf16 v[28:31], v[144:147], v[36:39], v[28:31]\n\t" \
"v_mfma_f32_16x16x32_bf16 v[32:35], v[36:39], v[148:151], v[32:35]\n\t" \
"23:\n\t" \
"s_cmp_lg_u32 %[hl], 3\n\t" \
"s_cbranch_scc1 24f\n\t" \
"v_mfma_f32_16x16x32_bf16 v[80:83], v[36:39], v[120:123], v[80:83]\n\t" \
"v_mfma_f32_16x16x32_bf16 v[84:87], v[36:39], v[124:127], v[84:87]\n\t" \
"v_mfma_f32_16x16x32_bf16 v[96:99], v[120:123], v[120:123], v[96:99]\n\t" \
"v_mfma_f32_16x16x32_bf16 v[100:103], v[124:127], v[124:127], v[100:103]\n\t" \
"24:\n\t" \
"s_waitcnt vmcnt(4) lgkmcnt(8)\n\t" \
"s_barrier\n\t" \
"v_mfma_f32_16x16x32_bf16 v[0:3], v[56:59], v[40:43], v[0:3]\n\t" \
"ds_read_b64_tr_b16 v[128:129], v90 offset:1024\n\t" \
"ds_read_b64_tr_b16 v[130:131], v90 offset:1536\n\t" \
"v_mfma_f32_16x16x32_bf16 v[4:7], v[56:59], v[44:47], v[4:7]\n\t" \
"ds_read_b128 v[112:115], v88\n\t" \
"ds_read_b128 v[116:119], v88 offset:8192\n\t" \
"global_load_lds_dwordx4 v104, s[92:93] offset:0\n\t" \
"v_mfma_f32_16x16x32_bf16 v[8:11], v[60:63], v[40:43], v[8:11]\n\t" \
"ds_read_b64_tr_b16 v[132:133], v90 offset:2048\n\t" \
"ds_read_b64_tr_b16 v[134:135], v90 offset:2560\n\t" \
"v_mfma_f32_16x16x32_bf16 v[12:15], v[60:63], v[44:47], v[12:15]\n\t" \
"s_waitcnt lgkmcnt(6)\n\t" \
"ds_read_b64_tr_b16 v[136:137], v90 offset:3072\n\t" \
"ds_read_b64_tr_b16 v[138:139], v90 offset:3584\n\t" \
"global_load_lds_dwordx4 v105, s[92:93] offset:1024\n\t" \
"v_mfma_f32_16x16x32_bf16 v[16:19], v[40:43], v[64:67], v[16:19]\n\t" \
"v_mfma_f32_16x16x32_bf16 v[20:23], v[44:47], v[64:67], v[20:23]\n\t" \
"s_cmp_lg_u32 %[m], 0\n\t" \
"s_cbranch_scc1 25f\n\t" \
"v_mfma_f32_16x16x32_bf16 v[24:27], v[56:59], v[36:39], v[24:27]\n\t" \
"v_mfma_f32_16x16x32_bf16 v[28:31], v[60:63], v[36:39], v[28:31]\n\t" \
"v_mfma_f32_16x16x32_bf16 v[32:35], v[36:39], v[64:67], v[32:35]\n\t" \
"25:\n\t" \
"s_cmp_lg_u32 %[hl], 0\n\t" \
"s_cbranch_scc1 26f\n\t" \
"v_mfma_f32_16x16x32_bf16 v[80:83], v[36:39], v[40:43], v[80:83]\n\t" \
"v_mfma_f32_16x16x32_bf16 v[84:87], v[36:39], v[44:47], v[84:87]\n\t" \
"v_mfma_f32_16x16x32_bf16 v[96:99], v[40:43], v[40:43], v[96:99]\n\t" \
"v_mfma_f32_16x16x32_bf16 v[100:103], v[44:47], v[44:47], v[100:103]\n\t" \
"26:\n\t" \
"v_mfma_f32_16x16x32_bf16 v[0:3], v[68:71], v[48:51], v[0:3]\n\t" \
"global_load_lds_dwordx4 v106, s[92:93] offset:2048\n\t" \
"ds_read_b64_tr_b16 v[140:141], v90 offset:17408\n\t" \
"ds_read_b64_tr_b16 v[142:143], v90 offset:17920\n\t" \
"v_mfma_f32_16x16x32_bf16 v[4:7], v[68:71], v[52:55], v[4:7]\n\t" \
"ds_read_b128 v[120:123], v89\n\t" \
"ds_read_b128 v[124:127], v89 offset:8192\n\t" \
"global_load_lds_dwordx4 v107, s[92:93] offset:3072\n\t" \
"v_mfma_f32_16x16x32_bf16 v[8:11], v[72:75], v[48:51], v[8:11]\n\t" \
"ds_read_b64_tr_b16 v[144:145], v90 offset:18432\n\t" \
"ds_read_b64_tr_b16 v[146:147], v90 offset:18944\n\t" \
"v_mfma_f32_16x16x32_bf16 v[12:15], v[72:75], v[52:55], v[12:15]\n\t" \
"s_waitcnt lgkmcnt(13)\n\t" \
"ds_read_b64_tr_b16 v[148:149], v90 offset:19456\n\t" \
"ds_read_b64_tr_b16 v[150:151], v90 offset:19968\n\t" \
"v_mfma_f32_16x16x32_bf16 v[16:19], v[48:51], v[76:79], v[16:19]\n\t" \
"s_add_u32 s92, s92, 0x20000\n\t" \
"s_addc_u32 s93, s93, 0\n\t" \
"s_add_u32 m0, %[lw], 0x0\n\t" \
"v_mfma_f32_16x16x32_bf16 v[20:23], v[52:55], v[76:79], v[20:23]\n\t" \
"s_cmp_lg_u32 %[m], 1\n\t" \
"s_cbranch_scc1 27f\n\t" \
"v_mfma_f32_16x16x32_bf16 v[24:27], v[68:71], v[36:39], v[24:27]\n\t" \
"v_mfma_f32_16x16x32_bf16 v[28:31], v[72:75], v[36:39], v[28:31]\n\t" \
"v_mfma_f32_16x16x32_bf16 v[32:35], v[36:39], v[76:79], v[32:35]\n\t" \
"27:\n\t" \
"s_cmp_lg_u32 %[hl], 1\n\t" \
"s_cbranch_scc1 28f\n\t" \
"v_mfma_f32_16x16x32_bf16 v[80:83], v[36:39], v[48:51], v[80:83]\n\t" \
"v_mfma_f32_16x16x32_bf16 v[84:87], v[36:39], v[52:55], v[84:87]\n\t" \
"v_mfma_f32_16x16x32_bf16 v[96:99], v[48:51], v[48:51], v[96:99]\n\t" \
"v_mfma_f32_16x16x32_bf16 v[100:103], v[52:55], v[52:55], v[100:103]\n\t" \
"28:\n\t" \
"s_waitcnt lgkmcnt(8)\n\t" \
"s_barrier\n\t" \
"v_mfma_f32_16x16x32_bf16 v[0:3], v[128:131], v[112:115], v[0:3]\n\t" \
"v_mfma_f32_16x16x32_bf16 v[4:7], v[128:131], v[116:119], v[4:7]\n\t" \
"v_mfma_f32_16x16x32_bf16 v[8:11], v[132:135], v[112:115], v[8:11]\n\t" \
"v_mfma_f32_16x16x32_bf16 v[12:15], v[132:135], v[116:119], v[12:15]\n\t" \
"s_waitcnt lgkmcnt(0)\n\t" \
"v_mfma_f32_16x16x32_bf16 v[16:19], v[112:115], v[136:139], v[16:19]\n\t" \
"v_mfma_f32_16x16x32_bf16 v[20:23], v[116:119], v[136:139], v[20:23]\n\t" \
"s_cmp_lg_u32 %[m], 0\n\t" \
"s_cbranch_scc1 29f\n\t" \
"v_mfma_f32_16x16x32_bf16 v[24:27], v[128:131], v[36:39], v[24:27]\n\t" \
"v_mfma_f32_16x16x32_bf16 v[28:31], v[132:135], v[36:39], v[28:31]\n\t" \
"v_mfma_f32_16x16x32_bf16 v[32:35], v[36:39], v[136:139], v[32:35]\n\t" \
"29:\n\t" \
"s_cmp_lg_u32 %[hl], 2\n\t" \
"s_cbranch_scc1 30f\n\t" \
"v_mfma_f32_16x16x32_bf16 v[80:83], v[36:39], v[112:115], v[80:83]\n\t" \
"v_mfma_f32_16x16x32_bf16 v[84:87], v[36:39], v[116:119], v[84:87]\n\t" \
"v_mfma_f32_16x16x32_bf16 v[96:99], v[112:115], v[112:115], v[96:99]\n\t" \
"v_mfma_f32_16x16x32_bf16 v[100:103], v[116:119], v[116:119], v[100:103]\n\t" \
"30:\n\t" \
"v_mfma_f32_16x16x32_bf16 v[0:3], v[140:143], v[120:123], v[0:3]\n\t" \
"v_mfma_f32_16x16x32_bf16 v[4:7], v[140:143], v[124:127], v[4:7]\n\t" \
"v_mfma_f32_16x16x32_bf16 v[8:11], v[144:147], v[120:123], v[8:11]\n\t" \
"v_mfma_f32_16x16x32_bf16 v[12:15], v[144:147], v[124:127], v[12:15]\n\t" \
"v_mfma_f32_16x16x32_bf16 v[16:19], v[120:123], v[148:151], v[16:19]\n\t" \
"v_mfma_f32_16x16x32_bf16 v[20:23], v[124:127], v[148:151], v[20:23]\n\t" \
"s_cmp_lg_u32 %[m], 1\n\t" \
"s_cbranch_scc1 31f\n\t" \
"v_mfma_f32_16x16x32_bf16 v[24:27], v[140:143], v[36:39], v[24:27]\n\t" \
"v_mfma_f32_16x16x32_bf16 v[28:31], v[144:147], v[36:39], v[28:31]\n\t" \
"v_mfma_f32_16x16x32_bf16 v[32:35], v[36:39], v[148:151], v[32:35]\n\t" \
"31:\n\t" \
"s_cmp_lg_u32 %[hl], 3\n\t" \
"s_cbranch_scc1 32f\n\t" \
"v_mfma_f32_16x16x32_bf16 v[80:83], v[36:39], v[120:123], v[80:83]\n\t" \
"v_mfma_f32_16x16x32_bf16 v[84:87], v[36:39], v[124:127], v[84:87]\n\t" \
"v_mfma_f32_16x16x32_bf16 v[96:99], v[120:123], v[120:123], v[96:99]\n\t" \
"v_mfma_f32_16x16x32_bf16 v[100:103], v[124:127], v[124:127], v[100:103]\n\t" \
"32:\n\t" \
"s_mov_b32 m0, %[keep]\n\t" \
"s_nop 6\n\t"

#define AT_FAST \
"s_cmp_lg_u32 %[slow], 0\n\t" \
"s_cbranch_scc1 9f\n\t" \
"s_mov_b32 %[keep], m0\n\t" \
"s_setprio 3\n\t" \
"v_exp_f32 v48, v48\n\t" \
"v_exp_f32 v49, v49\n\t" \
"v_smfmac_f32_32x32x32_bf16 v[16:31], v[96:99], v[72:79], v100\n\t" \
"v_exp_f32 v50, v50\n\t" \
"v_exp_f32 v51, v51\n\t" \
"v_add_u32 v103, v107, v102\n\t" \
"v_exp_f32 v52, v52\n\t" \
"v_exp_f32 v53, v53\n\t" \
"v_exp_f32 v54, v54\n\t" \
"v_mfma_f32_32x32x16_bf16 v[64:79], v[88:91], v[84:87], v[32:47]\n\t" \
"ds_read_b128 v[96:99], v102 offset:512\n\t" \
"ds_read_b128 v[88:91], v101 offset:1024\n\t" \
"v_exp_f32 v55, v55\n\t" \
"v_exp_f32 v56, v56\n\t" \
"v_exp_f32 v57, v57\n\t" \
"s_waitcnt lgkmcnt(2)\n\t" \
"v_smfmac_f32_32x32x32_bf16 v[0:15], v[92:95], v[48:55], v100\n\t" \
"v_exp_f32 v58, v58\n\t" \
"v_exp_f32 v59, v59\n\t" \
"ds_read_b64_tr_b16 v[224:225], v118 offset:0\n\t" \
"v_exp_f32 v60, v60\n\t" \
"ds_read_b64_tr_b16 v[226:227], v118 offset:2048\n\t" \
"v_exp_f32 v61, v61\n\t" \
"ds_read_b64_tr_b16 v[228:229], v118 offset:8192\n\t" \
"v_exp_f32 v62, v62\n\t" \
"ds_read_b64_tr_b16 v[230:231], v118 offset:10240\n\t" \
"v_exp_f32 v63, v63\n\t" \
"s_setprio 2\n\t" \
"v_exp_f32 v64, v64\n\t" \
"v_exp_f32 v65, v65\n\t" \
"s_waitcnt lgkmcnt(0)\n\t" \
"v_smfmac_f32_32x32x32_bf16 v[0:15], v[96:99], v[56:63], v100\n\t" \
"v_exp_f32 v66, v66\n\t" \
"v_exp_f32 v67, v67\n\t" \
"v_exp_f32 v68, v68\n\t" \
"v_exp_f32 v69, v69\n\t" \
"v_exp_f32 v70, v70\n\t" \
"v_mfma_f32_32x32x16_bf16 v[48:63], v[88:91], v[80:83], v[32:47]\n\t" \
"v_exp_f32 v71, v71\n\t" \
"v_exp_f32 v72, v72\n\t" \
"v_exp_f32 v73, v73\n\t" \
"v_smfmac_f32_32x32x32_bf16 v[16:31], v[92:95], v[64:71], v100\n\t" \
"ds_read_b128 v[92:95], v102 offset:1024\n\t" \
"v_exp_f32 v74, v74\n\t" \
"v_exp_f32 v75, v75\n\t" \
"ds_read_b64_tr_b16 v[232:233], v118 offset:16384\n\t" \
"v_exp_f32 v76, v76\n\t" \
"ds_read_b64_tr_b16 v[234:235], v118 offset:18432\n\t" \
"v_exp_f32 v77, v77\n\t" \
"ds_read_b64_tr_b16 v[236:237], v118 offset:24576\n\t" \
"v_exp_f32 v78, v78\n\t" \
"ds_read_b64_tr_b16 v[238:239], v118 offset:26624\n\t" \
"v_exp_f32 v79, v79\n\t" \
"s_setprio 1\n\t" \
"v_exp_f32 v48, v48\n\t" \
"v_exp_f32 v49, v49\n\t" \
"v_smfmac_f32_32x32x32_bf16 v[16:31], v[96:99], v[72:79], v100\n\t" \
"v_exp_f32 v50, v50\n\t" \
"v_exp_f32 v51, v51\n\t" \
"v_exp_f32 v52, v52\n\t" \
"v_exp_f32 v53, v53\n\t" \
"v_exp_f32 v54, v54\n\t" \
"v_mfma_f32_32x32x16_bf16 v[64:79], v[88:91], v[84:87], v[32:47]\n\t" \
"ds_read_b128 v[96:99], v102 offset:1536\n\t" \
"ds_read_b128 v[88:91], v101 offset:2048\n\t" \
"v_exp_f32 v55, v55\n\t" \
"v_exp_f32 v56, v56\n\t" \
"v_exp_f32 v57, v57\n\t" \
"s_waitcnt lgkmcnt(2)\n\t" \
"v_smfmac_f32_32x32x32_bf16 v[0:15], v[92:95], v[48:55], v100\n\t" \
"v_exp_f32 v58, v58\n\t" \
"v_exp_f32 v59, v59\n\t" \
"ds_read_b64_tr_b16 v[240:241], v118 offset:32768\n\t" \
"v_exp_f32 v60, v60\n\t" \
"ds_read_b64_tr_b16 v[242:243], v118 offset:34816\n\t" \
"v_exp_f32 v61, v61\n\t" \
"ds_read_b64_tr_b16 v[244:245], v118 offset:40960\n\t" \
"v_exp_f32 v62, v62\n\t" \
"ds_read_b64_tr_b16 v[246:247], v118 offset:43008\n\t" \
"v_exp_f32 v63, v63\n\t" \
"s_setprio 0\n\t" \
"v_exp_f32 v64, v64\n\t" \
"v_exp_f32 v65, v65\n\t" \
"s_waitcnt lgkmcnt(0)\n\t" \
"v_smfmac_f32_32x32x32_bf16 v[0:15], v[96:99], v[56:63], v100\n\t" \
"v_exp_f32 v66, v66\n\t" \
"v_exp_f32 v67, v67\n\t" \
"v_exp_f32 v68, v68\n\t" \
"v_exp_f32 v69, v69\n\t" \
"v_exp_f32 v70, v70\n\t" \
"v_mfma_f32_32x32x16_bf16 v[48:63], v[88:91], v[80:83], v[32:47]\n\t" \
"v_exp_f32 v71, v71\n\t" \
"v_exp_f32 v72, v72\n\t" \
"v_exp_f32 v73, v73\n\t" \
"v_smfmac_f32_32x32x32_bf16 v[16:31], v[92:95], v[64:71], v100\n\t" \
"ds_read_b128 v[92:95], v102 offset:2048\n\t" \
"v_exp_f32 v74, v74\n\t" \
"v_exp_f32 v75, v75\n\t" \
"ds_read_b64_tr_b16 v[248:249], v118 offset:49152\n\t" \
"v_exp_f32 v76, v76\n\t" \
"ds_read_b64_tr_b16 v[250:251], v118 offset:51200\n\t" \
"v_exp_f32 v77, v77\n\t" \
"ds_read_b64_tr_b16 v[252:253], v118 offset:57344\n\t" \
"v_exp_f32 v78, v78\n\t" \
"ds_read_b64_tr_b16 v[254:255], v118 offset:59392\n\t" \
"v_exp_f32 v79, v79\n\t" \
"s_waitcnt vmcnt(0) lgkmcnt(0)\n\t" \
"s_barrier\n\t" \
"s_setprio 3\n\t" \
"v_exp_f32 v48, v48\n\t" \
"v_exp_f32 v49, v49\n\t" \
"v_smfmac_f32_32x32x32_bf16 v[16:31], v[96:99], v[72:79], v100\n\t" \
"v_exp_f32 v50, v50\n\t" \
"s_add_u32 m0, %[kl], 0xa000\n\t" \
"v_exp_f32 v51, v51\n\t" \
"s_add_u32 s88, %[ko], 0x202000\n\t" \
"v_exp_f32 v52, v52\n\t" \
"buffer_load_dwordx4 v121, %[rs], s88 offen sc1 lds\n\t" \
"v_exp_f32 v53, v53\n\t" \
"v_exp_f32 v54, v54\n\t" \
"v_mfma_f32_32x32x16_bf16 v[64:79], v[88:91], v[84:87], v[32:47]\n\t" \
"ds_read_b128 v[96:99], v102 offset:2560\n\t" \
"ds_read_b128 v[88:91], v101 offset:3072\n\t" \
"v_exp_f32 v55, v55\n\t" \
"v_exp_f32 v56, v56\n\t" \
"v_exp_f32 v57, v57\n\t" \
"s_waitcnt lgkmcnt(2)\n\t" \
"v_smfmac_f32_32x32x32_bf16 v[0:15], v[92:95], v[48:55], v100\n\t" \
"v_exp_f32 v58, v58\n\t" \
"v_exp_f32 v59, v59\n\t" \
"s_add_u32 m0, %[kl], 0x8000\n\t" \
"v_exp_f32 v60, v60\n\t" \
"s_add_u32 s88, %[ko], 0x2000\n\t" \
"v_exp_f32 v61, v61\n\t" \
"buffer_load_dwordx4 v121, %[rs], s88 offen sc1 lds\n\t" \
"v_exp_f32 v62, v62\n\t" \
"v_exp_f32 v63, v63\n\t" \
"s_setprio 2\n\t" \
"v_exp_f32 v64, v64\n\t" \
"v_exp_f32 v65, v65\n\t" \
"s_waitcnt lgkmcnt(0)\n\t" \
"v_smfmac_f32_32x32x32_bf16 v[0:15], v[96:99], v[56:63], v100\n\t" \
"v_exp_f32 v66, v66\n\t" \
"s_add_u32 m0, %[kl], 0xe000\n\t" \
"v_exp_f32 v67, v67\n\t" \
"s_add_u32 s88, %[ko], 0x203000\n\t" \
"v_exp_f32 v68, v68\n\t" \
"buffer_load_dwordx4 v121, %[rs], s88 offen sc1 lds\n\t" \
"v_exp_f32 v69, v69\n\t" \
"v_exp_f32 v70, v70\n\t" \
"v_mfma_f32_32x32x16_bf16 v[48:63], v[88:91], v[80:83], v[32:47]\n\t" \
"v_exp_f32 v71, v71\n\t" \
"v_exp_f32 v72, v72\n\t" \
"v_exp_f32 v73, v73\n\t" \
"v_smfmac_f32_32x32x32_bf16 v[16:31], v[92:95], v[64:71], v100\n\t" \
"ds_read_b128 v[92:95], v102 offset:3072\n\t" \
"v_exp_f32 v74, v74\n\t" \
"v_exp_f32 v75, v75\n\t" \
"s_add_u32 m0, %[kl], 0xc000\n\t" \
"v_exp_f32 v76, v76\n\t" \
"s_add_u32 s88, %[ko], 0x3000\n\t" \
"v_exp_f32 v77, v77\n\t" \
"buffer_load_dwordx4 v121, %[rs], s88 offen sc1 lds\n\t" \
"v_exp_f32 v78, v78\n\t" \
"v_exp_f32 v79, v79\n\t" \
"s_setprio 1\n\t" \
"v_exp_f32 v48, v48\n\t" \
"v_exp_f32 v49, v49\n\t" \
"v_smfmac_f32_32x32x32_bf16 v[16:31], v[96:99], v[72:79], v100\n\t" \
"v_exp_f32 v50, v50\n\t" \
"v_exp_f32 v51, v51\n\t" \
"v_exp_f32 v52, v52\n\t" \
"v_exp_f32 v53, v53\n\t" \
"v_exp_f32 v54, v54\n\t" \
"v_mfma_f32_32x32x16_bf16 v[64:79], v[88:91], v[84:87], v[32:47]\n\t" \
"ds_read_b128 v[96:99], v102 offset:3584\n\t" \
"ds_read_b128 v[88:91], v101 offset:16384\n\t" \
"v_exp_f32 v55, v55\n\t" \
"v_exp_f32 v56, v56\n\t" \
"v_exp_f32 v57, v57\n\t" \
"s_waitcnt lgkmcnt(2)\n\t" \
"v_smfmac_f32_32x32x32_bf16 v[0:15], v[92:95], v[48:55], v100\n\t" \
"v_exp_f32 v58, v58\n\t" \
"v_exp_f32 v59, v59\n\t" \
"s_add_u32 m0, %[kl], 0x10000\n\t" \
"v_exp_f32 v60, v60\n\t" \
"s_add_u32 s88, %[ko], 0x4000\n\t" \
"v_exp_f32 v61, v61\n\t" \
"buffer_load_dwordx4 v121, %[rs], s88 offen sc1 lds\n\t" \
"v_exp_f32 v62, v62\n\t" \
"v_exp_f32 v63, v63\n\t" \
"s_setprio 0\n\t" \
"v_exp_f32 v64, v64\n\t" \
"v_exp_f32 v65, v65\n\t" \
"s_waitcnt lgkmcnt(0)\n\t" \
"v_smfmac_f32_32x32x32_bf16 v[0:15], v[96:99], v[56:63], v100\n\t" \
"v_exp_f32 v66, v66\n\t" \
"v_exp_f32 v67, v67\n\t" \
"v_exp_f32 v68, v68\n\t" \
"v_exp_f32 v69, v69\n\t" \
"v_exp_f32 v70, v70\n\t" \
"v_mfma_f32_32x32x16_bf16 v[48:63], v[88:91], v[80:83], v[32:47]\n\t" \
"v_exp_f32 v71, v71\n\t" \
"v_exp_f32 v72, v72\n\t" \
"v_exp_f32 v73, v73\n\t" \
"v_smfmac_f32_32x32x32_bf16 v[16:31], v[92:95], v[64:71], v100\n\t" \
"ds_read_b128 v[92:95], v103\n\t" \
"v_exp_f32 v74, v74\n\t" \
"v_exp_f32 v75, v75\n\t" \
"s_add_u32 m0, %[kl], 0x12000\n\t" \
"v_exp_f32 v76, v76\n\t" \
"s_add_u32 s88, %[ko], 0x204000\n\t" \
"v_exp_f32 v77, v77\n\t" \
"buffer_load_dwordx4 v121, %[rs], s88 offen sc1 lds\n\t" \
"v_exp_f32 v78, v78\n\t" \
"v_exp_f32 v79, v79\n\t" \
"s_setprio 3\n\t" \
"v_exp_f32 v48, v48\n\t" \
"v_exp_f32 v49, v49\n\t" \
"v_smfmac_f32_32x32x32_bf16 v[16:31], v[96:99], v[72:79], v100\n\t" \
"v_exp_f32 v50, v50\n\t" \
"v_exp_f32 v51, v51\n\t" \
"v_add_u32 v102, v107, v103\n\t" \
"v_exp_f32 v52, v52\n\t" \
"v_exp_f32 v53, v53\n\t" \
"v_exp_f32 v54, v54\n\t" \
"v_mfma_f32_32x32x16_bf16 v[64:79], v[88:91], v[84:87], v[32:47]\n\t" \
"ds_read_b128 v[96:99], v103 offset:512\n\t" \
"ds_read_b128 v[88:91], v101 offset:17408\n\t" \
"v_exp_f32 v55, v55\n\t" \
"v_exp_f32 v56, v56\n\t" \
"v_exp_f32 v57, v57\n\t" \
"s_waitcnt lgkmcnt(2)\n\t" \
"v_smfmac_f32_32x32x32_bf16 v[0:15], v[92:95], v[48:55], v100\n\t" \
"v_exp_f32 v58, v58\n\t" \
"v_exp_f32 v59, v59\n\t" \
"v_exp_f32 v60, v60\n\t" \
"v_exp_f32 v61, v61\n\t" \
"v_exp_f32 v62, v62\n\t" \
"v_exp_f32 v63, v63\n\t" \
"s_setprio 2\n\t" \
"v_exp_f32 v64, v64\n\t" \
"v_exp_f32 v65, v65\n\t" \
"s_waitcnt lgkmcnt(0)\n\t" \
"v_smfmac_f32_32x32x32_bf16 v[0:15], v[96:99], v[56:63], v100\n\t" \
"v_exp_f32 v66, v66\n\t" \
"v_exp_f32 v67, v67\n\t" \
"v_exp_f32 v68, v68\n\t" \
"v_exp_f32 v69, v69\n\t" \
"v_exp_f32 v70, v70\n\t" \
"v_mfma_f32_32x32x16_bf16 v[48:63], v[88:91], v[80:83], v[32:47]\n\t" \
"v_exp_f32 v71, v71\n\t" \
"v_exp_f32 v72, v72\n\t" \
"v_exp_f32 v73, v73\n\t" \
"v_smfmac_f32_32x32x32_bf16 v[16:31], v[92:95], v[64:71], v100\n\t" \
"ds_read_b128 v[92:95], v103 offset:1024\n\t" \
"v_exp_f32 v74, v74\n\t" \
"v_exp_f32 v75, v75\n\t" \
"v_exp_f32 v76, v76\n\t" \
"v_exp_f32 v77, v77\n\t" \
"v_exp_f32 v78, v78\n\t" \
"v_exp_f32 v79, v79\n\t" \
"s_setprio 1\n\t" \
"v_exp_f32 v48, v48\n\t" \
"v_exp_f32 v49, v49\n\t" \
"v_smfmac_f32_32x32x32_bf16 v[16:31], v[96:99], v[72:79], v100\n\t" \
"v_exp_f32 v50, v50\n\t" \
"v_exp_f32 v51, v51\n\t" \
"v_exp_f32 v52, v52\n\t" \
"v_exp_f32 v53, v53\n\t" \
"v_exp_f32 v54, v54\n\t" \
"v_mfma_f32_32x32x16_bf16 v[64:79], v[88:91], v[84:87], v[32:47]\n\t" \
"ds_read_b128 v[96:99], v103 offset:1536\n\t" \
"ds_read_b128 v[88:91], v101 offset:18432\n\t" \
"v_exp_f32 v55, v55\n\t" \
"v_exp_f32 v56, v56\n\t" \
"v_exp_f32 v57, v57\n\t" \
"s_waitcnt lgkmcnt(2)\n\t" \
"v_smfmac_f32_32x32x32_bf16 v[0:15], v[92:95], v[48:55], v100\n\t" \
"v_exp_f32 v58, v58\n\t" \
"v_exp_f32 v59, v59\n\t" \
"v_exp_f32 v60, v60\n\t" \
"v_exp_f32 v61, v61\n\t" \
"v_exp_f32 v62, v62\n\t" \
"v_exp_f32 v63, v63\n\t" \
"s_setprio 0\n\t" \
"v_exp_f32 v64, v64\n\t" \
"v_exp_f32 v65, v65\n\t" \
"s_waitcnt lgkmcnt(0)\n\t" \
"v_smfmac_f32_32x32x32_bf16 v[0:15], v[96:99], v[56:63], v100\n\t" \
"v_exp_f32 v66, v66\n\t" \
"v_exp_f32 v67, v67\n\t" \
"v_exp_f32 v68, v68\n\t" \
"v_exp_f32 v69, v69\n\t" \
"v_exp_f32 v70, v70\n\t" \
"v_mfma_f32_32x32x16_bf16 v[48:63], v[88:91], v[80:83], v[32:47]\n\t" \
"v_exp_f32 v71, v71\n\t" \
"v_exp_f32 v72, v72\n\t" \
"v_exp_f32 v73, v73\n\t" \
"v_smfmac_f32_32x32x32_bf16 v[16:31], v[92:95], v[64:71], v100\n\t" \
"ds_read_b128 v[92:95], v103 offset:2048\n\t" \
"v_exp_f32 v74, v74\n\t" \
"v_exp_f32 v75, v75\n\t" \
"v_exp_f32 v76, v76\n\t" \
"v_exp_f32 v77, v77\n\t" \
"v_exp_f32 v78, v78\n\t" \
"v_exp_f32 v79, v79\n\t" \
"s_waitcnt vmcnt(0)\n\t" \
"s_barrier\n\t" \
"s_setprio 3\n\t" \
"v_exp_f32 v48, v48\n\t" \
"v_exp_f32 v49, v49\n\t" \
"v_smfmac_f32_32x32x32_bf16 v[16:31], v[96:99], v[72:79], v100\n\t" \
"v_exp_f32 v50, v50\n\t" \
"v_exp_f32 v51, v51\n\t" \
"v_exp_f32 v52, v52\n\t" \
"v_exp_f32 v53, v53\n\t" \
"v_exp_f32 v54, v54\n\t" \
"v_mfma_f32_32x32x16_bf16 v[64:79], v[88:91], v[84:87], v[32:47]\n\t" \
"ds_read_b128 v[96:99], v103 offset:2560\n\t" \
"ds_read_b128 v[88:91], v101 offset:19456\n\t" \
"v_exp_f32 v55, v55\n\t" \
"v_exp_f32 v56, v56\n\t" \
"v_exp_f32 v57, v57\n\t" \
"s_waitcnt lgkmcnt(2)\n\t" \
"v_smfmac_f32_32x32x32_bf16 v[0:15], v[92:95], v[48:55], v100\n\t" \
"v_exp_f32 v58, v58\n\t" \
"v_exp_f32 v59, v59\n\t" \
"s_add_u32 m0, %[kl], 0x14000\n\t" \
"v_exp_f32 v60, v60\n\t" \
"s_add_u32 s88, %[ko], 0x5000\n\t" \
"v_exp_f32 v61, v61\n\t" \
"buffer_load_dwordx4 v121, %[rs], s88 offen sc1 lds\n\t" \
"v_exp_f32 v62, v62\n\t" \
"v_exp_f32 v63, v63\n\t" \
"s_setprio 2\n\t" \
"v_exp_f32 v64, v64\n\t" \
"v_exp_f32 v65, v65\n\t" \
"s_waitcnt lgkmcnt(0)\n\t" \
"v_smfmac_f32_32x32x32_bf16 v[0:15], v[96:99], v[56:63], v100\n\t" \
"v_exp_f32 v66, v66\n\t" \
"v_exp_f32 v67, v67\n\t" \
"v_exp_f32 v68, v68\n\t" \
"v_exp_f32 v69, v69\n\t" \
"v_exp_f32 v70, v70\n\t" \
"v_mfma_f32_32x32x16_bf16 v[48:63], v[88:91], v[80:83], v[32:47]\n\t" \
"v_exp_f32 v71, v71\n\t" \
"v_exp_f32 v72, v72\n\t" \
"v_exp_f32 v73, v73\n\t" \
"v_smfmac_f32_32x32x32_bf16 v[16:31], v[92:95], v[64:71], v100\n\t" \
"ds_read_b128 v[92:95], v103 offset:3072\n\t" \
"v_exp_f32 v74, v74\n\t" \
"v_exp_f32 v75, v75\n\t" \
"s_add_u32 m0, %[kl], 0x16000\n\t" \
"v_exp_f32 v76, v76\n\t" \
"s_add_u32 s88, %[ko], 0x205000\n\t" \
"v_exp_f32 v77, v77\n\t" \
"buffer_load_dwordx4 v121, %[rs], s88 offen sc1 lds\n\t" \
"v_exp_f32 v78, v78\n\t" \
"v_exp_f32 v79, v79\n\t" \
"s_setprio 1\n\t" \
"v_exp_f32 v48, v48\n\t" \
"v_exp_f32 v49, v49\n\t" \
"v_smfmac_f32_32x32x32_bf16 v[16:31], v[96:99], v[72:79], v100\n\t" \
"v_exp_f32 v50, v50\n\t" \
"v_exp_f32 v51, v51\n\t" \
"v_exp_f32 v52, v52\n\t" \
"v_exp_f32 v53, v53\n\t" \
"v_exp_f32 v54, v54\n\t" \
"v_mfma_f32_32x32x16_bf16 v[64:79], v[88:91], v[84:87], v[32:47]\n\t" \
"ds_read_b128 v[96:99], v103 offset:3584\n\t" \
"ds_read_b128 v[88:91], v101 offset:32768\n\t" \
"v_exp_f32 v55, v55\n\t" \
"v_exp_f32 v56, v56\n\t" \
"v_exp_f32 v57, v57\n\t" \
"s_waitcnt lgkmcnt(2)\n\t" \
"v_smfmac_f32_32x32x32_bf16 v[0:15], v[92:95], v[48:55], v100\n\t" \
"v_exp_f32 v58, v58\n\t" \
"v_exp_f32 v59, v59\n\t" \
"v_exp_f32 v60, v60\n\t" \
"v_exp_f32 v61, v61\n\t" \
"v_exp_f32 v62, v62\n\t" \
"v_exp_f32 v63, v63\n\t" \
"s_setprio 0\n\t" \
"v_exp_f32 v64, v64\n\t" \
"v_exp_f32 v65, v65\n\t" \
"s_waitcnt lgkmcnt(0)\n\t" \
"v_smfmac_f32_32x32x32_bf16 v[0:15], v[96:99], v[56:63], v100\n\t" \
"v_exp_f32 v66, v66\n\t" \
"v_exp_f32 v67, v67\n\t" \
"v_exp_f32 v68, v68\n\t" \
"v_exp_f32 v69, v69\n\t" \
"v_exp_f32 v70, v70\n\t" \
"v_mfma_f32_32x32x16_bf16 v[48:63], v[88:91], v[80:83], v[32:47]\n\t" \
"v_exp_f32 v71, v71\n\t" \
"v_exp_f32 v72, v72\n\t" \
"v_exp_f32 v73, v73\n\t" \
"v_smfmac_f32_32x32x32_bf16 v[16:31], v[92:95], v[64:71], v100\n\t" \
"ds_read_b128 v[92:95], v102\n\t" \
"v_exp_f32 v74, v74\n\t" \
"v_exp_f32 v75, v75\n\t" \
"s_add_u32 m0, %[kl], 0x18000\n\t" \
"v_exp_f32 v76, v76\n\t" \
"s_add_u32 s88, %[ko], 0x6000\n\t" \
"v_exp_f32 v77, v77\n\t" \
"buffer_load_dwordx4 v121, %[rs], s88 offen sc1 lds\n\t" \
"v_exp_f32 v78, v78\n\t" \
"v_exp_f32 v79, v79\n\t" \
"s_setprio 3\n\t" \
"v_exp_f32 v48, v48\n\t" \
"v_exp_f32 v49, v49\n\t" \
"v_smfmac_f32_32x32x32_bf16 v[16:31], v[96:99], v[72:79], v100\n\t" \
"v_exp_f32 v50, v50\n\t" \
"global_load_dwordx2 v[188:189], v126, %[x0]\n\t" \
"v_exp_f32 v51, v51\n\t" \
"v_add_u32 v103, v107, v102\n\t" \
"v_exp_f32 v52, v52\n\t" \
"v_exp_f32 v53, v53\n\t" \
"v_exp_f32 v54, v54\n\t" \
"v_mfma_f32_32x32x16_bf16 v[64:79], v[88:91], v[84:87], v[32:47]\n\t" \
"ds_read_b128 v[96:99], v102 offset:512\n\t" \
"ds_read_b128 v[88:91], v101 offset:33792\n\t" \
"v_exp_f32 v55, v55\n\t" \
"v_exp_f32 v56, v56\n\t" \
"v_exp_f32 v57, v57\n\t" \
"s_waitcnt lgkmcnt(2)\n\t" \
"v_smfmac_f32_32x32x32_bf16 v[0:15], v[92:95], v[48:55], v100\n\t" \
"v_exp_f32 v58, v58\n\t" \
"v_exp_f32 v59, v59\n\t" \
"s_add_u32 m0, %[kl], 0x1a000\n\t" \
"v_exp_f32 v60, v60\n\t" \
"s_add_u32 s88, %[ko], 0x206000\n\t" \
"v_exp_f32 v61, v61\n\t" \
"buffer_load_dwordx4 v121, %[rs], s88 offen sc1 lds\n\t" \
"v_exp_f32 v62, v62\n\t" \
"v_exp_f32 v63, v63\n\t" \
"s_setprio 2\n\t" \
"v_exp_f32 v64, v64\n\t" \
"v_exp_f32 v65, v65\n\t" \
"s_waitcnt lgkmcnt(0)\n\t" \
"v_smfmac_f32_32x32x32_bf16 v[0:15], v[96:99], v[56:63], v100\n\t" \
"v_exp_f32 v66, v66\n\t" \
"v_exp_f32 v67, v67\n\t" \
"v_exp_f32 v68, v68\n\t" \
"v_exp_f32 v69, v69\n\t" \
"v_exp_f32 v70, v70\n\t" \
"v_mfma_f32_32x32x16_bf16 v[48:63], v[88:91], v[80:83], v[32:47]\n\t" \
"v_exp_f32 v71, v71\n\t" \
"v_exp_f32 v72, v72\n\t" \
"v_exp_f32 v73, v73\n\t" \
"v_smfmac_f32_32x32x32_bf16 v[16:31], v[92:95], v[64:71], v100\n\t" \
"ds_read_b128 v[92:95], v102 offset:1024\n\t" \
"v_exp_f32 v74, v74\n\t" \
"v_exp_f32 v75, v75\n\t" \
"global_load_dwordx2 v[190:191], v126, %[x1]\n\t" \
"v_exp_f32 v76, v76\n\t" \
"v_exp_f32 v77, v77\n\t" \
"v_exp_f32 v78, v78\n\t" \
"v_exp_f32 v79, v79\n\t" \
"s_setprio 1\n\t" \
"v_exp_f32 v48, v48\n\t" \
"v_exp_f32 v49, v49\n\t" \
"v_smfmac_f32_32x32x32_bf16 v[16:31], v[96:99], v[72:79], v100\n\t" \
"v_exp_f32 v50, v50\n\t" \
"v_exp_f32 v51, v51\n\t" \
"v_exp_f32 v52, v52\n\t" \
"v_exp_f32 v53, v53\n\t" \
"v_exp_f32 v54, v54\n\t" \
"v_mfma_f32_32x32x16_bf16 v[64:79], v[88:91], v[84:87], v[32:47]\n\t" \
"ds_read_b128 v[96:99], v102 offset:1536\n\t" \
"ds_read_b128 v[88:91], v101 offset:34816\n\t" \
"v_exp_f32 v55, v55\n\t" \
"v_exp_f32 v56, v56\n\t" \
"v_exp_f32 v57, v57\n\t" \
"s_waitcnt lgkmcnt(2)\n\t" \
"v_smfmac_f32_32x32x32_bf16 v[0:15], v[92:95], v[48:55], v100\n\t" \
"v_exp_f32 v58, v58\n\t" \
"v_exp_f32 v59, v59\n\t" \
"s_add_u32 m0, %[kl], 0x1c000\n\t" \
"v_exp_f32 v60, v60\n\t" \
"s_add_u32 s88, %[ko], 0x7000\n\t" \
"v_exp_f32 v61, v61\n\t" \
"buffer_load_dwordx4 v121, %[rs], s88 offen sc1 lds\n\t" \
"v_exp_f32 v62, v62\n\t" \
"v_exp_f32 v63, v63\n\t" \
"s_setprio 0\n\t" \
"v_exp_f32 v64, v64\n\t" \
"v_exp_f32 v65, v65\n\t" \
"s_waitcnt lgkmcnt(0)\n\t" \
"v_smfmac_f32_32x32x32_bf16 v[0:15], v[96:99], v[56:63], v100\n\t" \
"v_exp_f32 v66, v66\n\t" \
"v_exp_f32 v67, v67\n\t" \
"v_exp_f32 v68, v68\n\t" \
"v_exp_f32 v69, v69\n\t" \
"v_exp_f32 v70, v70\n\t" \
"v_mfma_f32_32x32x16_bf16 v[48:63], v[88:91], v[80:83], v[32:47]\n\t" \
"v_exp_f32 v71, v71\n\t" \
"v_exp_f32 v72, v72\n\t" \
"v_exp_f32 v73, v73\n\t" \
"v_smfmac_f32_32x32x32_bf16 v[16:31], v[92:95], v[64:71], v100\n\t" \
"ds_read_b128 v[92:95], v102 offset:2048\n\t" \
"v_exp_f32 v74, v74\n\t" \
"v_exp_f32 v75, v75\n\t" \
"s_add_u32 m0, %[kl], 0x1e000\n\t" \
"v_exp_f32 v76, v76\n\t" \
"s_add_u32 s88, %[ko], 0x207000\n\t" \
"v_exp_f32 v77, v77\n\t" \
"buffer_load_dwordx4 v121, %[rs], s88 offen sc1 lds\n\t" \
"v_exp_f32 v78, v78\n\t" \
"v_exp_f32 v79, v79\n\t" \
"s_setprio 3\n\t" \
"v_exp_f32 v48, v48\n\t" \
"v_exp_f32 v49, v49\n\t" \
"v_smfmac_f32_32x32x32_bf16 v[16:31], v[96:99], v[72:79], v100\n\t" \
"v_exp_f32 v50, v50\n\t" \
"v_exp_f32 v51, v51\n\t" \
"v_exp_f32 v52, v52\n\t" \
"v_exp_f32 v53, v53\n\t" \
"v_exp_f32 v54, v54\n\t" \
"v_mfma_f32_32x32x16_bf16 v[64:79], v[88:91], v[84:87], v[32:47]\n\t" \
"ds_read_b128 v[96:99], v102 offset:2560\n\t" \
"ds_read_b128 v[88:91], v101 offset:35840\n\t" \
"v_exp_f32 v55, v55\n\t" \
"v_exp_f32 v56, v56\n\t" \
"v_exp_f32 v57, v57\n\t" \
"s_waitcnt lgkmcnt(2)\n\t" \
"v_smfmac_f32_32x32x32_bf16 v[0:15], v[92:95], v[48:55], v100\n\t" \
"v_exp_f32 v58, v58\n\t" \
"v_exp_f32 v59, v59\n\t" \
"global_load_dword v104, v105, %[wu]\n\t" \
"v_exp_f32 v60, v60\n\t" \
"v_exp_f32 v61, v61\n\t" \
"v_exp_f32 v62, v62\n\t" \
"v_exp_f32 v63, v63\n\t" \
"s_setprio 2\n\t" \
"v_exp_f32 v64, v64\n\t" \
"v_exp_f32 v65, v65\n\t" \
"s_waitcnt lgkmcnt(0)\n\t" \
"v_smfmac_f32_32x32x32_bf16 v[0:15], v[96:99], v[56:63], v100\n\t" \
"v_exp_f32 v66, v66\n\t" \
"v_exp_f32 v67, v67\n\t" \
"v_exp_f32 v68, v68\n\t" \
"v_exp_f32 v69, v69\n\t" \
"v_exp_f32 v70, v70\n\t" \
"v_mfma_f32_32x32x16_bf16 v[48:63], v[88:91], v[80:83], v[32:47]\n\t" \
"v_exp_f32 v71, v71\n\t" \
"v_exp_f32 v72, v72\n\t" \
"v_exp_f32 v73, v73\n\t" \
"v_smfmac_f32_32x32x32_bf16 v[16:31], v[92:95], v[64:71], v100\n\t" \
"ds_read_b128 v[92:95], v102 offset:3072\n\t" \
"v_exp_f32 v74, v74\n\t" \
"v_exp_f32 v75, v75\n\t" \
"v_exp_f32 v76, v76\n\t" \
"v_exp_f32 v77, v77\n\t" \
"v_exp_f32 v78, v78\n\t" \
"v_exp_f32 v79, v79\n\t" \
"s_setprio 1\n\t" \
"v_exp_f32 v48, v48\n\t" \
"v_exp_f32 v49, v49\n\t" \
"v_smfmac_f32_32x32x32_bf16 v[16:31], v[96:99], v[72:79], v100\n\t" \
"v_exp_f32 v50, v50\n\t" \
"v_exp_f32 v51, v51\n\t" \
"v_exp_f32 v52, v52\n\t" \
"v_exp_f32 v53, v53\n\t" \
"v_exp_f32 v54, v54\n\t" \
"v_mfma_f32_32x32x16_bf16 v[64:79], v[88:91], v[84:87], v[32:47]\n\t" \
"ds_read_b128 v[96:99], v102 offset:3584\n\t" \
"ds_read_b128 v[88:91], v101 offset:49152\n\t" \
"v_exp_f32 v55, v55\n\t" \
"v_exp_f32 v56, v56\n\t" \
"v_exp_f32 v57, v57\n\t" \
"s_waitcnt lgkmcnt(2)\n\t" \
"v_smfmac_f32_32x32x32_bf16 v[0:15], v[92:95], v[48:55], v100\n\t" \
"v_exp_f32 v58, v58\n\t" \
"v_exp_f32 v59, v59\n\t" \
"v_exp_f32 v60, v60\n\t" \
"v_exp_f32 v61, v61\n\t" \
"v_exp_f32 v62, v62\n\t" \
"v_exp_f32 v63, v63\n\t" \
"s_setprio 0\n\t" \
"v_exp_f32 v64, v64\n\t" \
"v_exp_f32 v65, v65\n\t" \
"s_waitcnt lgkmcnt(0)\n\t" \
"v_smfmac_f32_32x32x32_bf16 v[0:15], v[96:99], v[56:63], v100\n\t" \
"v_exp_f32 v66, v66\n\t" \
"v_exp_f32 v67, v67\n\t" \
"v_add_u32 v101, 0xc000, v101\n\t" \
"v_exp_f32 v68, v68\n\t" \
"v_exp_f32 v69, v69\n\t" \
"v_exp_f32 v70, v70\n\t" \
"v_mfma_f32_32x32x16_bf16 v[48:63], v[88:91], v[80:83], v[32:47]\n\t" \
"v_exp_f32 v71, v71\n\t" \
"v_exp_f32 v72, v72\n\t" \
"v_exp_f32 v73, v73\n\t" \
"v_smfmac_f32_32x32x32_bf16 v[16:31], v[92:95], v[64:71], v100\n\t" \
"ds_read_b128 v[92:95], v103\n\t" \
"v_exp_f32 v74, v74\n\t" \
"v_exp_f32 v75, v75\n\t" \
"v_exp_f32 v76, v76\n\t" \
"v_exp_f32 v77, v77\n\t" \
"v_exp_f32 v78, v78\n\t" \
"v_exp_f32 v79, v79\n\t" \
"s_setprio 3\n\t" \
"v_exp_f32 v48, v48\n\t" \
"v_exp_f32 v49, v49\n\t" \
"v_smfmac_f32_32x32x32_bf16 v[16:31], v[96:99], v[72:79], v100\n\t" \
"v_exp_f32 v50, v50\n\t" \
"v_exp_f32 v51, v51\n\t" \
"v_add_u32 v102, v107, v103\n\t" \
"v_exp_f32 v52, v52\n\t" \
"v_exp_f32 v53, v53\n\t" \
"v_exp_f32 v54, v54\n\t" \
"v_mfma_f32_32x32x16_bf16 v[64:79], v[88:91], v[84:87], v[32:47]\n\t" \
"ds_read_b128 v[96:99], v103 offset:512\n\t" \
"ds_read_b128 v[88:91], v101 offset:1024\n\t" \
"v_exp_f32 v55, v55\n\t" \
"v_exp_f32 v56, v56\n\t" \
"v_exp_f32 v57, v57\n\t" \
"s_waitcnt lgkmcnt(2)\n\t" \
"v_smfmac_f32_32x32x32_bf16 v[0:15], v[92:95], v[48:55], v100\n\t" \
"v_exp_f32 v58, v58\n\t" \
"v_exp_f32 v59, v59\n\t" \
"v_exp_f32 v60, v60\n\t" \
"v_exp_f32 v61, v61\n\t" \
"v_exp_f32 v62, v62\n\t" \
"v_exp_f32 v63, v63\n\t" \
"s_setprio 2\n\t" \
"v_exp_f32 v64, v64\n\t" \
"v_exp_f32 v65, v65\n\t" \
"s_waitcnt lgkmcnt(0)\n\t" \
"v_smfmac_f32_32x32x32_bf16 v[0:15], v[96:99], v[56:63], v100\n\t" \
"v_exp_f32 v66, v66\n\t" \
"v_exp_f32 v67, v67\n\t" \
"v_exp_f32 v68, v68\n\t" \
"v_exp_f32 v69, v69\n\t" \
"v_exp_f32 v70, v70\n\t" \
"v_mfma_f32_32x32x16_bf16 v[48:63], v[88:91], v[80:83], v[32:47]\n\t" \
"v_exp_f32 v71, v71\n\t" \
"v_exp_f32 v72, v72\n\t" \
"v_exp_f32 v73, v73\n\t" \
"v_smfmac_f32_32x32x32_bf16 v[16:31], v[92:95], v[64:71], v100\n\t" \
"ds_read_b128 v[92:95], v103 offset:1024\n\t" \
"v_exp_f32 v74, v74\n\t" \
"v_exp_f32 v75, v75\n\t" \
"v_exp_f32 v76, v76\n\t" \
"v_exp_f32 v77, v77\n\t" \
"v_exp_f32 v78, v78\n\t" \
"v_exp_f32 v79, v79\n\t" \
"s_setprio 1\n\t" \
"v_exp_f32 v48, v48\n\t" \
"v_exp_f32 v49, v49\n\t" \
"v_smfmac_f32_32x32x32_bf16 v[16:31], v[96:99], v[72:79], v100\n\t" \
"v_exp_f32 v50, v50\n\t" \
"v_exp_f32 v51, v51\n\t" \
"v_exp_f32 v52, v52\n\t" \
"v_exp_f32 v53, v53\n\t" \
"v_exp_f32 v54, v54\n\t" \
"v_mfma_f32_32x32x16_bf16 v[64:79], v[88:91], v[84:87], v[32:47]\n\t" \
"ds_read_b128 v[96:99], v103 offset:1536\n\t" \
"ds_read_b128 v[88:91], v101 offset:2048\n\t" \
"v_exp_f32 v55, v55\n\t" \
"v_exp_f32 v56, v56\n\t" \
"v_exp_f32 v57, v57\n\t" \
"s_waitcnt lgkmcnt(2)\n\t" \
"v_smfmac_f32_32x32x32_bf16 v[0:15], v[92:95], v[48:55], v100\n\t" \
"v_exp_f32 v58, v58\n\t" \
"v_exp_f32 v59, v59\n\t" \
"v_exp_f32 v60, v60\n\t" \
"v_exp_f32 v61, v61\n\t" \
"v_exp_f32 v62, v62\n\t" \
"v_exp_f32 v63, v63\n\t" \
"s_setprio 0\n\t" \
"v_exp_f32 v64, v64\n\t" \
"v_exp_f32 v65, v65\n\t" \
"s_waitcnt lgkmcnt(0)\n\t" \
"v_smfmac_f32_32x32x32_bf16 v[0:15], v[96:99], v[56:63], v100\n\t" \
"v_exp_f32 v66, v66\n\t" \
"v_exp_f32 v67, v67\n\t" \
"v_exp_f32 v68, v68\n\t" \
"v_exp_f32 v69, v69\n\t" \
"v_exp_f32 v70, v70\n\t" \
"v_mfma_f32_32x32x16_bf16 v[48:63], v[88:91], v[80:83], v[32:47]\n\t" \
"v_exp_f32 v71, v71\n\t" \
"v_exp_f32 v72, v72\n\t" \
"v_exp_f32 v73, v73\n\t" \
"v_smfmac_f32_32x32x32_bf16 v[16:31], v[92:95], v[64:71], v100\n\t" \
"ds_read_b128 v[92:95], v103 offset:2048\n\t" \
"v_exp_f32 v74, v74\n\t" \
"v_exp_f32 v75, v75\n\t" \
"v_exp_f32 v76, v76\n\t" \
"v_exp_f32 v77, v77\n\t" \
"v_exp_f32 v78, v78\n\t" \
"v_exp_f32 v79, v79\n\t" \
"s_waitcnt vmcnt(0)\n\t" \
"s_barrier\n\t" \
"s_setprio 3\n\t" \
"v_exp_f32 v48, v48\n\t" \
"v_exp_f32 v49, v49\n\t" \
"v_smfmac_f32_32x32x32_bf16 v[16:31], v[96:99], v[72:79], v100\n\t" \
"v_exp_f32 v50, v50\n\t" \
"v_exp_f32 v51, v51\n\t" \
"v_exp_f32 v52, v52\n\t" \
"v_exp_f32 v53, v53\n\t" \
"v_exp_f32 v54, v54\n\t" \
"v_mfma_f32_32x32x16_bf16 v[64:79], v[88:91], v[84:87], v[32:47]\n\t" \
"ds_read_b128 v[96:99], v103 offset:2560\n\t" \
"ds_read_b128 v[88:91], v101 offset:3072\n\t" \
"v_exp_f32 v55, v55\n\t" \
"v_exp_f32 v56, v56\n\t" \
"v_exp_f32 v57, v57\n\t" \
"s_waitcnt lgkmcnt(2)\n\t" \
"v_smfmac_f32_32x32x32_bf16 v[0:15], v[92:95], v[48:55], v100\n\t" \
"v_exp_f32 v58, v58\n\t" \
"v_exp_f32 v59, v59\n\t" \
"s_add_u32 m0, %[gl], 0\n\t" \
"v_exp_f32 v60, v60\n\t" \
"v_exp_f32 v61, v61\n\t" \
"global_load_lds_dwordx4 v122, %[g0]\n\t" \
"v_exp_f32 v62, v62\n\t" \
"v_exp_f32 v63, v63\n\t" \
"s_setprio 2\n\t" \
"v_exp_f32 v64, v64\n\t" \
"v_exp_f32 v65, v65\n\t" \
"s_waitcnt lgkmcnt(0)\n\t" \
"v_smfmac_f32_32x32x32_bf16 v[0:15], v[96:99], v[56:63], v100\n\t" \
"v_exp_f32 v66, v66\n\t" \
"v_exp_f32 v67, v67\n\t" \
"v_exp_f32 v68, v68\n\t" \
"v_exp_f32 v69, v69\n\t" \
"v_exp_f32 v70, v70\n\t" \
"v_mfma_f32_32x32x16_bf16 v[48:63], v[88:91], v[80:83], v[32:47]\n\t" \
"v_exp_f32 v71, v71\n\t" \
"v_exp_f32 v72, v72\n\t" \
"v_exp_f32 v73, v73\n\t" \
"v_smfmac_f32_32x32x32_bf16 v[16:31], v[92:95], v[64:71], v100\n\t" \
"ds_read_b128 v[92:95], v103 offset:3072\n\t" \
"v_exp_f32 v74, v74\n\t" \
"v_exp_f32 v75, v75\n\t" \
"s_add_u32 m0, %[gl], 1024\n\t" \
"v_exp_f32 v76, v76\n\t" \
"v_exp_f32 v77, v77\n\t" \
"global_load_lds_dwordx4 v123, %[g0]\n\t" \
"v_exp_f32 v78, v78\n\t" \
"v_exp_f32 v79, v79\n\t" \
"s_setprio 1\n\t" \
"v_exp_f32 v48, v48\n\t" \
"v_exp_f32 v49, v49\n\t" \
"v_smfmac_f32_32x32x32_bf16 v[16:31], v[96:99], v[72:79], v100\n\t" \
"v_exp_f32 v50, v50\n\t" \
"v_exp_f32 v51, v51\n\t" \
"v_exp_f32 v52, v52\n\t" \
"v_exp_f32 v53, v53\n\t" \
"v_exp_f32 v54, v54\n\t" \
"v_mfma_f32_32x32x16_bf16 v[64:79], v[88:91], v[84:87], v[32:47]\n\t" \
"ds_read_b128 v[96:99], v103 offset:3584\n\t" \
"ds_read_b128 v[88:91], v101 offset:16384\n\t" \
"v_exp_f32 v55, v55\n\t" \
"v_exp_f32 v56, v56\n\t" \
"v_exp_f32 v57, v57\n\t" \
"s_waitcnt lgkmcnt(2)\n\t" \
"v_smfmac_f32_32x32x32_bf16 v[0:15], v[92:95], v[48:55], v100\n\t" \
"v_exp_f32 v58, v58\n\t" \
"v_exp_f32 v59, v59\n\t" \
"v_exp_f32 v60, v60\n\t" \
"v_exp_f32 v61, v61\n\t" \
"v_exp_f32 v62, v62\n\t" \
"v_exp_f32 v63, v63\n\t" \
"s_setprio 0\n\t" \
"v_exp_f32 v64, v64\n\t" \
"v_exp_f32 v65, v65\n\t" \
"s_waitcnt lgkmcnt(0)\n\t" \
"v_smfmac_f32_32x32x32_bf16 v[0:15], v[96:99], v[56:63], v100\n\t" \
"v_exp_f32 v66, v66\n\t" \
"v_exp_f32 v67, v67\n\t" \
"v_exp_f32 v68, v68\n\t" \
"v_exp_f32 v69, v69\n\t" \
"v_exp_f32 v70, v70\n\t" \
"v_mfma_f32_32x32x16_bf16 v[48:63], v[88:91], v[80:83], v[32:47]\n\t" \
"v_exp_f32 v71, v71\n\t" \
"v_exp_f32 v72, v72\n\t" \
"v_exp_f32 v73, v73\n\t" \
"v_smfmac_f32_32x32x32_bf16 v[16:31], v[92:95], v[64:71], v100\n\t" \
"ds_read_b128 v[92:95], v102\n\t" \
"v_exp_f32 v74, v74\n\t" \
"v_exp_f32 v75, v75\n\t" \
"s_add_u32 m0, %[gl], 2048\n\t" \
"v_exp_f32 v76, v76\n\t" \
"v_exp_f32 v77, v77\n\t" \
"global_load_lds_dwordx4 v124, %[g0]\n\t" \
"v_exp_f32 v78, v78\n\t" \
"v_exp_f32 v79, v79\n\t" \
"s_setprio 3\n\t" \
"v_exp_f32 v48, v48\n\t" \
"v_exp_f32 v49, v49\n\t" \
"v_smfmac_f32_32x32x32_bf16 v[16:31], v[96:99], v[72:79], v100\n\t" \
"v_exp_f32 v50, v50\n\t" \
"v_exp_f32 v51, v51\n\t" \
"v_add_u32 v103, v107, v102\n\t" \
"v_exp_f32 v52, v52\n\t" \
"v_exp_f32 v53, v53\n\t" \
"v_exp_f32 v54, v54\n\t" \
"v_mfma_f32_32x32x16_bf16 v[64:79], v[88:91], v[84:87], v[32:47]\n\t" \
"ds_read_b128 v[96:99], v102 offset:512\n\t" \
"ds_read_b128 v[88:91], v101 offset:17408\n\t" \
"v_exp_f32 v55, v55\n\t" \
"v_exp_f32 v56, v56\n\t" \
"v_exp_f32 v57, v57\n\t" \
"s_waitcnt lgkmcnt(2)\n\t" \
"v_smfmac_f32_32x32x32_bf16 v[0:15], v[92:95], v[48:55], v100\n\t" \
"v_exp_f32 v58, v58\n\t" \
"v_exp_f32 v59, v59\n\t" \
"s_add_u32 m0, %[gl], 3072\n\t" \
"v_exp_f32 v60, v60\n\t" \
"v_exp_f32 v61, v61\n\t" \
"global_load_lds_dwordx4 v122, %[g1]\n\t" \
"v_exp_f32 v62, v62\n\t" \
"v_exp_f32 v63, v63\n\t" \
"s_setprio 2\n\t" \
"v_exp_f32 v64, v64\n\t" \
"v_exp_f32 v65, v65\n\t" \
"s_waitcnt lgkmcnt(0)\n\t" \
"v_smfmac_f32_32x32x32_bf16 v[0:15], v[96:99], v[56:63], v100\n\t" \
"v_exp_f32 v66, v66\n\t" \
"v_exp_f32 v67, v67\n\t" \
"v_exp_f32 v68, v68\n\t" \
"v_exp_f32 v69, v69\n\t" \
"v_exp_f32 v70, v70\n\t" \
"v_mfma_f32_32x32x16_bf16 v[48:63], v[88:91], v[80:83], v[32:47]\n\t" \
"v_exp_f32 v71, v71\n\t" \
"v_exp_f32 v72, v72\n\t" \
"v_exp_f32 v73, v73\n\t" \
"v_smfmac_f32_32x32x32_bf16 v[16:31], v[92:95], v[64:71], v100\n\t" \
"ds_read_b128 v[92:95], v102 offset:1024\n\t" \
"v_exp_f32 v74, v74\n\t" \
"v_exp_f32 v75, v75\n\t" \
"v_exp_f32 v76, v76\n\t" \
"v_exp_f32 v77, v77\n\t" \
"v_exp_f32 v78, v78\n\t" \
"v_exp_f32 v79, v79\n\t" \
"s_setprio 1\n\t" \
"v_exp_f32 v48, v48\n\t" \
"v_exp_f32 v49, v49\n\t" \
"v_smfmac_f32_32x32x32_bf16 v[16:31], v[96:99], v[72:79], v100\n\t" \
"v_exp_f32 v50, v50\n\t" \
"v_exp_f32 v51, v51\n\t" \
"v_exp_f32 v52, v52\n\t" \
"v_exp_f32 v53, v53\n\t" \
"v_exp_f32 v54, v54\n\t" \
"v_mfma_f32_32x32x16_bf16 v[64:79], v[88:91], v[84:87], v[32:47]\n\t" \
"ds_read_b128 v[96:99], v102 offset:1536\n\t" \
"ds_read_b128 v[88:91], v101 offset:18432\n\t" \
"v_exp_f32 v55, v55\n\t" \
"v_exp_f32 v56, v56\n\t" \
"v_exp_f32 v57, v57\n\t" \
"s_waitcnt lgkmcnt(2)\n\t" \
"v_smfmac_f32_32x32x32_bf16 v[0:15], v[92:95], v[48:55], v100\n\t" \
"v_exp_f32 v58, v58\n\t" \
"v_exp_f32 v59, v59\n\t" \
"s_add_u32 m0, %[gl], 4096\n\t" \
"v_exp_f32 v60, v60\n\t" \
"v_exp_f32 v61, v61\n\t" \
"global_load_lds_dwordx4 v123, %[g1]\n\t" \
"v_exp_f32 v62, v62\n\t" \
"v_exp_f32 v63, v63\n\t" \
"s_setprio 0\n\t" \
"v_exp_f32 v64, v64\n\t" \
"v_exp_f32 v65, v65\n\t" \
"s_waitcnt lgkmcnt(0)\n\t" \
"v_smfmac_f32_32x32x32_bf16 v[0:15], v[96:99], v[56:63], v100\n\t" \
"v_exp_f32 v66, v66\n\t" \
"v_exp_f32 v67, v67\n\t" \
"v_exp_f32 v68, v68\n\t" \
"v_exp_f32 v69, v69\n\t" \
"v_exp_f32 v70, v70\n\t" \
"v_mfma_f32_32x32x16_bf16 v[48:63], v[88:91], v[80:83], v[32:47]\n\t" \
"v_exp_f32 v71, v71\n\t" \
"v_exp_f32 v72, v72\n\t" \
"v_exp_f32 v73, v73\n\t" \
"v_smfmac_f32_32x32x32_bf16 v[16:31], v[92:95], v[64:71], v100\n\t" \
"ds_read_b128 v[92:95], v102 offset:2048\n\t" \
"v_exp_f32 v74, v74\n\t" \
"v_exp_f32 v75, v75\n\t" \
"s_add_u32 m0, %[gl], 5120\n\t" \
"v_exp_f32 v76, v76\n\t" \
"v_exp_f32 v77, v77\n\t" \
"global_load_lds_dwordx4 v124, %[g1]\n\t" \
"v_exp_f32 v78, v78\n\t" \
"v_exp_f32 v79, v79\n\t" \
"s_setprio 3\n\t" \
"v_exp_f32 v48, v48\n\t" \
"v_exp_f32 v49, v49\n\t" \
"v_smfmac_f32_32x32x32_bf16 v[16:31], v[96:99], v[72:79], v100\n\t" \
"v_exp_f32 v50, v50\n\t" \
"v_exp_f32 v51, v51\n\t" \
"v_exp_f32 v52, v52\n\t" \
"v_exp_f32 v53, v53\n\t" \
"v_exp_f32 v54, v54\n\t" \
"v_mfma_f32_32x32x16_bf16 v[64:79], v[88:91], v[84:87], v[32:47]\n\t" \
"ds_read_b128 v[96:99], v102 offset:2560\n\t" \
"ds_read_b128 v[88:91], v101 offset:19456\n\t" \
"v_exp_f32 v55, v55\n\t" \
"v_exp_f32 v56, v56\n\t" \
"v_exp_f32 v57, v57\n\t" \
"s_waitcnt lgkmcnt(2)\n\t" \
"v_smfmac_f32_32x32x32_bf16 v[0:15], v[92:95], v[48:55], v100\n\t" \
"v_exp_f32 v58, v58\n\t" \
"v_exp_f32 v59, v59\n\t" \
"global_load_dword v104, v105, %[wd]\n\t" \
"v_exp_f32 v60, v60\n\t" \
"v_exp_f32 v61, v61\n\t" \
"v_exp_f32 v62, v62\n\t" \
"v_exp_f32 v63, v63\n\t" \
"s_setprio 2\n\t" \
"v_exp_f32 v64, v64\n\t" \
"v_exp_f32 v65, v65\n\t" \
"s_waitcnt lgkmcnt(0)\n\t" \
"v_smfmac_f32_32x32x32_bf16 v[0:15], v[96:99], v[56:63], v100\n\t" \
"v_exp_f32 v66, v66\n\t" \
"v_exp_f32 v67, v67\n\t" \
"v_exp_f32 v68, v68\n\t" \
"v_exp_f32 v69, v69\n\t" \
"v_exp_f32 v70, v70\n\t" \
"v_mfma_f32_32x32x16_bf16 v[48:63], v[88:91], v[80:83], v[32:47]\n\t" \
"v_exp_f32 v71, v71\n\t" \
"v_exp_f32 v72, v72\n\t" \
"v_exp_f32 v73, v73\n\t" \
"v_smfmac_f32_32x32x32_bf16 v[16:31], v[92:95], v[64:71], v100\n\t" \
"ds_read_b128 v[92:95], v102 offset:3072\n\t" \
"v_exp_f32 v74, v74\n\t" \
"v_exp_f32 v75, v75\n\t" \
"v_exp_f32 v76, v76\n\t" \
"v_exp_f32 v77, v77\n\t" \
"v_exp_f32 v78, v78\n\t" \
"v_exp_f32 v79, v79\n\t" \
"s_setprio 1\n\t" \
"v_exp_f32 v48, v48\n\t" \
"v_exp_f32 v49, v49\n\t" \
"v_smfmac_f32_32x32x32_bf16 v[16:31], v[96:99], v[72:79], v100\n\t" \
"v_exp_f32 v50, v50\n\t" \
"v_exp_f32 v51, v51\n\t" \
"v_exp_f32 v52, v52\n\t" \
"v_exp_f32 v53, v53\n\t" \
"v_exp_f32 v54, v54\n\t" \
"v_mfma_f32_32x32x16_bf16 v[64:79], v[88:91], v[84:87], v[32:47]\n\t" \
"ds_read_b128 v[96:99], v102 offset:3584\n\t" \
"ds_read_b128 v[88:91], v101 offset:32768\n\t" \
"v_exp_f32 v55, v55\n\t" \
"v_exp_f32 v56, v56\n\t" \
"v_exp_f32 v57, v57\n\t" \
"s_waitcnt lgkmcnt(2)\n\t" \
"v_smfmac_f32_32x32x32_bf16 v[0:15], v[92:95], v[48:55], v100\n\t" \
"v_exp_f32 v58, v58\n\t" \
"v_exp_f32 v59, v59\n\t" \
"v_exp_f32 v60, v60\n\t" \
"v_exp_f32 v61, v61\n\t" \
"v_exp_f32 v62, v62\n\t" \
"v_exp_f32 v63, v63\n\t" \
"s_setprio 0\n\t" \
"v_exp_f32 v64, v64\n\t" \
"v_exp_f32 v65, v65\n\t" \
"s_waitcnt lgkmcnt(0)\n\t" \
"v_smfmac_f32_32x32x32_bf16 v[0:15], v[96:99], v[56:63], v100\n\t" \
"v_exp_f32 v66, v66\n\t" \
"v_exp_f32 v67, v67\n\t" \
"v_exp_f32 v68, v68\n\t" \
"v_exp_f32 v69, v69\n\t" \
"v_exp_f32 v70, v70\n\t" \
"v_mfma_f32_32x32x16_bf16 v[48:63], v[88:91], v[80:83], v[32:47]\n\t" \
"v_exp_f32 v71, v71\n\t" \
"v_exp_f32 v72, v72\n\t" \
"v_exp_f32 v73, v73\n\t" \
"v_smfmac_f32_32x32x32_bf16 v[16:31], v[92:95], v[64:71], v100\n\t" \
"ds_read_b128 v[92:95], v103\n\t" \
"v_exp_f32 v74, v74\n\t" \
"v_exp_f32 v75, v75\n\t" \
"v_exp_f32 v76, v76\n\t" \
"v_exp_f32 v77, v77\n\t" \
"v_exp_f32 v78, v78\n\t" \
"v_exp_f32 v79, v79\n\t" \
"s_setprio 3\n\t" \
"v_exp_f32 v48, v48\n\t" \
"v_exp_f32 v49, v49\n\t" \
"v_smfmac_f32_32x32x32_bf16 v[16:31], v[96:99], v[72:79], v100\n\t" \
"v_exp_f32 v50, v50\n\t" \
"v_exp_f32 v51, v51\n\t" \
"v_add_u32 v102, v107, v103\n\t" \
"v_exp_f32 v52, v52\n\t" \
"v_exp_f32 v53, v53\n\t" \
"v_exp_f32 v54, v54\n\t" \
"v_mfma_f32_32x32x16_bf16 v[64:79], v[88:91], v[84:87], v[32:47]\n\t" \
"ds_read_b128 v[96:99], v103 offset:512\n\t" \
"ds_read_b128 v[88:91], v101 offset:33792\n\t" \
"v_exp_f32 v55, v55\n\t" \
"v_exp_f32 v56, v56\n\t" \
"v_exp_f32 v57, v57\n\t" \
"s_waitcnt lgkmcnt(2)\n\t" \
"v_smfmac_f32_32x32x32_bf16 v[0:15], v[92:95], v[48:55], v100\n\t" \
"v_exp_f32 v58, v58\n\t" \
"v_exp_f32 v59, v59\n\t" \
"v_exp_f32 v60, v60\n\t" \
"v_exp_f32 v61, v61\n\t" \
"v_exp_f32 v62, v62\n\t" \
"v_exp_f32 v63, v63\n\t" \
"s_setprio 2\n\t" \
"v_exp_f32 v64, v64\n\t" \
"v_exp_f32 v65, v65\n\t" \
"s_waitcnt lgkmcnt(0)\n\t" \
"v_smfmac_f32_32x32x32_bf16 v[0:15], v[96:99], v[56:63], v100\n\t" \
"v_exp_f32 v66, v66\n\t" \
"v_exp_f32 v67, v67\n\t" \
"v_exp_f32 v68, v68\n\t" \
"v_exp_f32 v69, v69\n\t" \
"v_exp_f32 v70, v70\n\t" \
"v_mfma_f32_32x32x16_bf16 v[48:63], v[88:91], v[80:83], v[32:47]\n\t" \
"v_exp_f32 v71, v71\n\t" \
"v_exp_f32 v72, v72\n\t" \
"v_exp_f32 v73, v73\n\t" \
"v_smfmac_f32_32x32x32_bf16 v[16:31], v[92:95], v[64:71], v100\n\t" \
"ds_read_b128 v[92:95], v103 offset:1024\n\t" \
"v_exp_f32 v74, v74\n\t" \
"v_exp_f32 v75, v75\n\t" \
"v_exp_f32 v76, v76\n\t" \
"v_exp_f32 v77, v77\n\t" \
"v_exp_f32 v78, v78\n\t" \
"v_exp_f32 v79, v79\n\t" \
"s_setprio 1\n\t" \
"v_exp_f32 v48, v48\n\t" \
"v_exp_f32 v49, v49\n\t" \
"v_smfmac_f32_32x32x32_bf16 v[16:31], v[96:99], v[72:79], v100\n\t" \
"v_exp_f32 v50, v50\n\t" \
"v_exp_f32 v51, v51\n\t" \
"v_exp_f32 v52, v52\n\t" \
"v_exp_f32 v53, v53\n\t" \
"v_exp_f32 v54, v54\n\t" \
"v_mfma_f32_32x32x16_bf16 v[64:79], v[88:91], v[84:87], v[32:47]\n\t" \
"ds_read_b128 v[96:99], v103 offset:1536\n\t" \
"ds_read_b128 v[88:91], v101 offset:34816\n\t" \
"v_exp_f32 v55, v55\n\t" \
"v_exp_f32 v56, v56\n\t" \
"v_exp_f32 v57, v57\n\t" \
"s_waitcnt lgkmcnt(2)\n\t" \
"v_smfmac_f32_32x32x32_bf16 v[0:15], v[92:95], v[48:55], v100\n\t" \
"v_exp_f32 v58, v58\n\t" \
"v_exp_f32 v59, v59\n\t" \
"buffer_load_dwordx4 v[128:131], v106, %[wr], %[wo] offen\n\t" \
"v_exp_f32 v60, v60\n\t" \
"v_exp_f32 v61, v61\n\t" \
"v_exp_f32 v62, v62\n\t" \
"v_exp_f32 v63, v63\n\t" \
"s_setprio 0\n\t" \
"v_exp_f32 v64, v64\n\t" \
"v_exp_f32 v65, v65\n\t" \
"s_waitcnt lgkmcnt(0)\n\t" \
"v_smfmac_f32_32x32x32_bf16 v[0:15], v[96:99], v[56:63], v100\n\t" \
"v_exp_f32 v66, v66\n\t" \
"v_exp_f32 v67, v67\n\t" \
"v_exp_f32 v68, v68\n\t" \
"v_exp_f32 v69, v69\n\t" \
"v_exp_f32 v70, v70\n\t" \
"v_mfma_f32_32x32x16_bf16 v[48:63], v[88:91], v[80:83], v[32:47]\n\t" \
"v_exp_f32 v71, v71\n\t" \
"v_exp_f32 v72, v72\n\t" \
"v_exp_f32 v73, v73\n\t" \
"v_smfmac_f32_32x32x32_bf16 v[16:31], v[92:95], v[64:71], v100\n\t" \
"ds_read_b128 v[92:95], v103 offset:2048\n\t" \
"v_exp_f32 v74, v74\n\t" \
"v_exp_f32 v75, v75\n\t" \
"s_add_u32 %[wo], %[wo], 0x4000\n\t" \
"v_exp_f32 v76, v76\n\t" \
"buffer_load_dwordx4 v[132:135], v106, %[wr], %[wo] offen\n\t" \
"v_exp_f32 v77, v77\n\t" \
"v_exp_f32 v78, v78\n\t" \
"v_exp_f32 v79, v79\n\t" \
"s_setprio 3\n\t" \
"v_exp_f32 v48, v48\n\t" \
"v_exp_f32 v49, v49\n\t" \
"v_smfmac_f32_32x32x32_bf16 v[16:31], v[96:99], v[72:79], v100\n\t" \
"v_exp_f32 v50, v50\n\t" \
"v_exp_f32 v51, v51\n\t" \
"v_exp_f32 v52, v52\n\t" \
"v_exp_f32 v53, v53\n\t" \
"v_exp_f32 v54, v54\n\t" \
"v_mfma_f32_32x32x16_bf16 v[64:79], v[88:91], v[84:87], v[32:47]\n\t" \
"ds_read_b128 v[96:99], v103 offset:2560\n\t" \
"ds_read_b128 v[88:91], v101 offset:35840\n\t" \
"v_exp_f32 v55, v55\n\t" \
"v_exp_f32 v56, v56\n\t" \
"v_exp_f32 v57, v57\n\t" \
"s_waitcnt lgkmcnt(2)\n\t" \
"v_smfmac_f32_32x32x32_bf16 v[0:15], v[92:95], v[48:55], v100\n\t" \
"v_exp_f32 v58, v58\n\t" \
"v_exp_f32 v59, v59\n\t" \
"s_add_u32 %[wo], %[wo], 0x4000\n\t" \
"v_exp_f32 v60, v60\n\t" \
"buffer_load_dwordx4 v[136:139], v106, %[wr], %[wo] offen\n\t" \
"v_exp_f32 v61, v61\n\t" \
"v_exp_f32 v62, v62\n\t" \
"v_exp_f32 v63, v63\n\t" \
"s_setprio 2\n\t" \
"v_exp_f32 v64, v64\n\t" \
"v_exp_f32 v65, v65\n\t" \
"s_waitcnt lgkmcnt(0)\n\t" \
"v_smfmac_f32_32x32x32_bf16 v[0:15], v[96:99], v[56:63], v100\n\t" \
"v_exp_f32 v66, v66\n\t" \
"v_exp_f32 v67, v67\n\t" \
"v_exp_f32 v68, v68\n\t" \
"v_exp_f32 v69, v69\n\t" \
"v_exp_f32 v70, v70\n\t" \
"v_mfma_f32_32x32x16_bf16 v[48:63], v[88:91], v[80:83], v[32:47]\n\t" \
"v_exp_f32 v71, v71\n\t" \
"v_exp_f32 v72, v72\n\t" \
"v_exp_f32 v73, v73\n\t" \
"v_smfmac_f32_32x32x32_bf16 v[16:31], v[92:95], v[64:71], v100\n\t" \
"ds_read_b128 v[92:95], v103 offset:3072\n\t" \
"v_exp_f32 v74, v74\n\t" \
"v_exp_f32 v75, v75\n\t" \
"v_exp_f32 v76, v76\n\t" \
"v_exp_f32 v77, v77\n\t" \
"v_exp_f32 v78, v78\n\t" \
"v_exp_f32 v79, v79\n\t" \
"s_setprio 1\n\t" \
"v_exp_f32 v48, v48\n\t" \
"v_exp_f32 v49, v49\n\t" \
"v_smfmac_f32_32x32x32_bf16 v[16:31], v[96:99], v[72:79], v100\n\t" \
"v_exp_f32 v50, v50\n\t" \
"v_exp_f32 v51, v51\n\t" \
"v_exp_f32 v52, v52\n\t" \
"v_exp_f32 v53, v53\n\t" \
"v_exp_f32 v54, v54\n\t" \
"v_mfma_f32_32x32x16_bf16 v[64:79], v[88:91], v[84:87], v[32:47]\n\t" \
"ds_read_b128 v[96:99], v103 offset:3584\n\t" \
"ds_read_b128 v[88:91], v101 offset:49152\n\t" \
"v_exp_f32 v55, v55\n\t" \
"v_exp_f32 v56, v56\n\t" \
"v_exp_f32 v57, v57\n\t" \
"s_waitcnt lgkmcnt(2)\n\t" \
"v_smfmac_f32_32x32x32_bf16 v[0:15], v[92:95], v[48:55], v100\n\t" \
"v_exp_f32 v58, v58\n\t" \
"v_exp_f32 v59, v59\n\t" \
"s_add_u32 %[wo], %[wo], 0x4000\n\t" \
"v_exp_f32 v60, v60\n\t" \
"buffer_load_dwordx4 v[140:143], v106, %[wr], %[wo] offen\n\t" \
"v_exp_f32 v61, v61\n\t" \
"v_exp_f32 v62, v62\n\t" \
"v_exp_f32 v63, v63\n\t" \
"s_setprio 0\n\t" \
"v_exp_f32 v64, v64\n\t" \
"v_exp_f32 v65, v65\n\t" \
"s_waitcnt lgkmcnt(0)\n\t" \
"v_smfmac_f32_32x32x32_bf16 v[0:15], v[96:99], v[56:63], v100\n\t" \
"v_exp_f32 v66, v66\n\t" \
"v_exp_f32 v67, v67\n\t" \
"v_add_u32 v101, 0xc000, v101\n\t" \
"v_exp_f32 v68, v68\n\t" \
"v_exp_f32 v69, v69\n\t" \
"v_exp_f32 v70, v70\n\t" \
"v_mfma_f32_32x32x16_bf16 v[48:63], v[88:91], v[80:83], v[32:47]\n\t" \
"v_exp_f32 v71, v71\n\t" \
"v_exp_f32 v72, v72\n\t" \
"v_exp_f32 v73, v73\n\t" \
"v_smfmac_f32_32x32x32_bf16 v[16:31], v[92:95], v[64:71], v100\n\t" \
"ds_read_b128 v[92:95], v102\n\t" \
"v_exp_f32 v74, v74\n\t" \
"v_exp_f32 v75, v75\n\t" \
"s_add_u32 %[wo], %[wo], 0x4000\n\t" \
"v_exp_f32 v76, v76\n\t" \
"buffer_load_dwordx4 v[144:147], v106, %[wr], %[wo] offen\n\t" \
"v_exp_f32 v77, v77\n\t" \
"v_exp_f32 v78, v78\n\t" \
"v_exp_f32 v79, v79\n\t" \
"s_setprio 3\n\t" \
"v_exp_f32 v48, v48\n\t" \
"v_exp_f32 v49, v49\n\t" \
"v_smfmac_f32_32x32x32_bf16 v[16:31], v[96:99], v[72:79], v100\n\t" \
"v_exp_f32 v50, v50\n\t" \
"v_exp_f32 v51, v51\n\t" \
"v_add_u32 v103, v107, v102\n\t" \
"v_exp_f32 v52, v52\n\t" \
"v_exp_f32 v53, v53\n\t" \
"v_exp_f32 v54, v54\n\t" \
"v_mfma_f32_32x32x16_bf16 v[64:79], v[88:91], v[84:87], v[32:47]\n\t" \
"ds_read_b128 v[96:99], v102 offset:512\n\t" \
"ds_read_b128 v[88:91], v101 offset:1024\n\t" \
"v_exp_f32 v55, v55\n\t" \
"v_exp_f32 v56, v56\n\t" \
"v_exp_f32 v57, v57\n\t" \
"s_waitcnt lgkmcnt(2)\n\t" \
"v_smfmac_f32_32x32x32_bf16 v[0:15], v[92:95], v[48:55], v100\n\t" \
"v_exp_f32 v58, v58\n\t" \
"v_exp_f32 v59, v59\n\t" \
"v_exp_f32 v60, v60\n\t" \
"v_exp_f32 v61, v61\n\t" \
"v_exp_f32 v62, v62\n\t" \
"v_exp_f32 v63, v63\n\t" \
"s_setprio 2\n\t" \
"v_exp_f32 v64, v64\n\t" \
"v_exp_f32 v65, v65\n\t" \
"s_waitcnt lgkmcnt(0)\n\t" \
"v_smfmac_f32_32x32x32_bf16 v[0:15], v[96:99], v[56:63], v100\n\t" \
"v_exp_f32 v66, v66\n\t" \
"v_exp_f32 v67, v67\n\t" \
"v_exp_f32 v68, v68\n\t" \
"v_exp_f32 v69, v69\n\t" \
"v_exp_f32 v70, v70\n\t" \
"v_mfma_f32_32x32x16_bf16 v[48:63], v[88:91], v[80:83], v[32:47]\n\t" \
"v_exp_f32 v71, v71\n\t" \
"v_exp_f32 v72, v72\n\t" \
"v_exp_f32 v73, v73\n\t" \
"v_smfmac_f32_32x32x32_bf16 v[16:31], v[92:95], v[64:71], v100\n\t" \
"ds_read_b128 v[92:95], v102 offset:1024\n\t" \
"v_exp_f32 v74, v74\n\t" \
"v_exp_f32 v75, v75\n\t" \
"s_add_u32 %[wo], %[wo], 0x4000\n\t" \
"v_exp_f32 v76, v76\n\t" \
"buffer_load_dwordx4 v[148:151], v106, %[wr], %[wo] offen\n\t" \
"v_exp_f32 v77, v77\n\t" \
"v_exp_f32 v78, v78\n\t" \
"v_exp_f32 v79, v79\n\t" \
"s_setprio 1\n\t" \
"v_exp_f32 v48, v48\n\t" \
"v_exp_f32 v49, v49\n\t" \
"v_smfmac_f32_32x32x32_bf16 v[16:31], v[96:99], v[72:79], v100\n\t" \
"v_exp_f32 v50, v50\n\t" \
"v_exp_f32 v51, v51\n\t" \
"v_exp_f32 v52, v52\n\t" \
"v_exp_f32 v53, v53\n\t" \
"v_exp_f32 v54, v54\n\t" \
"v_mfma_f32_32x32x16_bf16 v[64:79], v[88:91], v[84:87], v[32:47]\n\t" \
"ds_read_b128 v[96:99], v102 offset:1536\n\t" \
"ds_read_b128 v[88:91], v101 offset:2048\n\t" \
"v_exp_f32 v55, v55\n\t" \
"v_exp_f32 v56, v56\n\t" \
"v_exp_f32 v57, v57\n\t" \
"s_waitcnt lgkmcnt(2)\n\t" \
"v_smfmac_f32_32x32x32_bf16 v[0:15], v[92:95], v[48:55], v100\n\t" \
"v_exp_f32 v58, v58\n\t" \
"v_exp_f32 v59, v59\n\t" \
"s_add_u32 %[wo], %[wo], 0x4000\n\t" \
"v_exp_f32 v60, v60\n\t" \
"buffer_load_dwordx4 v[152:155], v106, %[wr], %[wo] offen\n\t" \
"v_exp_f32 v61, v61\n\t" \
"v_exp_f32 v62, v62\n\t" \
"v_exp_f32 v63, v63\n\t" \
"s_setprio 0\n\t" \
"v_exp_f32 v64, v64\n\t" \
"v_exp_f32 v65, v65\n\t" \
"s_waitcnt lgkmcnt(0)\n\t" \
"v_smfmac_f32_32x32x32_bf16 v[0:15], v[96:99], v[56:63], v100\n\t" \
"v_exp_f32 v66, v66\n\t" \
"v_exp_f32 v67, v67\n\t" \
"v_exp_f32 v68, v68\n\t" \
"v_exp_f32 v69, v69\n\t" \
"v_exp_f32 v70, v70\n\t" \
"v_mfma_f32_32x32x16_bf16 v[48:63], v[88:91], v[80:83], v[32:47]\n\t" \
"v_exp_f32 v71, v71\n\t" \
"v_exp_f32 v72, v72\n\t" \
"v_exp_f32 v73, v73\n\t" \
"v_smfmac_f32_32x32x32_bf16 v[16:31], v[92:95], v[64:71], v100\n\t" \
"ds_read_b128 v[92:95], v102 offset:2048\n\t" \
"v_exp_f32 v74, v74\n\t" \
"v_exp_f32 v75, v75\n\t" \
"s_add_u32 %[wo], %[wo], 0x4000\n\t" \
"v_exp_f32 v76, v76\n\t" \
"buffer_load_dwordx4 v[156:159], v106, %[wr], %[wo] offen\n\t" \
"v_exp_f32 v77, v77\n\t" \
"v_exp_f32 v78, v78\n\t" \
"v_exp_f32 v79, v79\n\t" \
"s_setprio 3\n\t" \
"v_exp_f32 v48, v48\n\t" \
"v_exp_f32 v49, v49\n\t" \
"v_smfmac_f32_32x32x32_bf16 v[16:31], v[96:99], v[72:79], v100\n\t" \
"v_exp_f32 v50, v50\n\t" \
"v_exp_f32 v51, v51\n\t" \
"v_exp_f32 v52, v52\n\t" \
"v_exp_f32 v53, v53\n\t" \
"v_exp_f32 v54, v54\n\t" \
"v_mfma_f32_32x32x16_bf16 v[64:79], v[88:91], v[84:87], v[32:47]\n\t" \
"ds_read_b128 v[96:99], v102 offset:2560\n\t" \
"ds_read_b128 v[88:91], v101 offset:3072\n\t" \
"v_exp_f32 v55, v55\n\t" \
"v_exp_f32 v56, v56\n\t" \
"v_exp_f32 v57, v57\n\t" \
"s_waitcnt lgkmcnt(2)\n\t" \
"v_smfmac_f32_32x32x32_bf16 v[0:15], v[92:95], v[48:55], v100\n\t" \
"v_exp_f32 v58, v58\n\t" \
"v_exp_f32 v59, v59\n\t" \
"v_exp_f32 v60, v60\n\t" \
"v_exp_f32 v61, v61\n\t" \
"v_exp_f32 v62, v62\n\t" \
"v_exp_f32 v63, v63\n\t" \
"s_setprio 2\n\t" \
"v_exp_f32 v64, v64\n\t" \
"v_exp_f32 v65, v65\n\t" \
"s_waitcnt lgkmcnt(0)\n\t" \
"v_smfmac_f32_32x32x32_bf16 v[0:15], v[96:99], v[56:63], v100\n\t" \
"v_exp_f32 v66, v66\n\t" \
"v_exp_f32 v67, v67\n\t" \
"v_exp_f32 v68, v68\n\t" \
"v_exp_f32 v69, v69\n\t" \
"v_exp_f32 v70, v70\n\t" \
"v_mfma_f32_32x32x16_bf16 v[48:63], v[88:91], v[80:83], v[32:47]\n\t" \
"v_exp_f32 v71, v71\n\t" \
"v_exp_f32 v72, v72\n\t" \
"v_exp_f32 v73, v73\n\t" \
"v_smfmac_f32_32x32x32_bf16 v[16:31], v[92:95], v[64:71], v100\n\t" \
"ds_read_b128 v[92:95], v102 offset:3072\n\t" \
"v_exp_f32 v74, v74\n\t" \
"v_exp_f32 v75, v75\n\t" \
"s_add_u32 %[wo], %[wo], 0x4000\n\t" \
"v_exp_f32 v76, v76\n\t" \
"buffer_load_dwordx4 v[160:163], v106, %[wr], %[wo] offen\n\t" \
"v_exp_f32 v77, v77\n\t" \
"v_exp_f32 v78, v78\n\t" \
"v_exp_f32 v79, v79\n\t" \
"s_setprio 1\n\t" \
"v_exp_f32 v48, v48\n\t" \
"v_exp_f32 v49, v49\n\t" \
"v_smfmac_f32_32x32x32_bf16 v[16:31], v[96:99], v[72:79], v100\n\t" \
"v_exp_f32 v50, v50\n\t" \
"v_exp_f32 v51, v51\n\t" \
"v_exp_f32 v52, v52\n\t" \
"v_exp_f32 v53, v53\n\t" \
"v_exp_f32 v54, v54\n\t" \
"v_mfma_f32_32x32x16_bf16 v[64:79], v[88:91], v[84:87], v[32:47]\n\t" \
"ds_read_b128 v[96:99], v102 offset:3584\n\t" \
"ds_read_b128 v[88:91], v101 offset:16384\n\t" \
"v_exp_f32 v55, v55\n\t" \
"v_exp_f32 v56, v56\n\t" \
"v_exp_f32 v57, v57\n\t" \
"s_waitcnt lgkmcnt(2)\n\t" \
"v_smfmac_f32_32x32x32_bf16 v[0:15], v[92:95], v[48:55], v100\n\t" \
"v_exp_f32 v58, v58\n\t" \
"v_exp_f32 v59, v59\n\t" \
"s_add_u32 %[wo], %[wo], 0x4000\n\t" \
"v_exp_f32 v60, v60\n\t" \
"buffer_load_dwordx4 v[164:167], v106, %[wr], %[wo] offen\n\t" \
"v_exp_f32 v61, v61\n\t" \
"v_exp_f32 v62, v62\n\t" \
"v_exp_f32 v63, v63\n\t" \
"s_setprio 0\n\t" \
"v_exp_f32 v64, v64\n\t" \
"v_exp_f32 v65, v65\n\t" \
"s_waitcnt lgkmcnt(0)\n\t" \
"v_smfmac_f32_32x32x32_bf16 v[0:15], v[96:99], v[56:63], v100\n\t" \
"v_exp_f32 v66, v66\n\t" \
"v_exp_f32 v67, v67\n\t" \
"v_exp_f32 v68, v68\n\t" \
"v_exp_f32 v69, v69\n\t" \
"v_exp_f32 v70, v70\n\t" \
"v_mfma_f32_32x32x16_bf16 v[48:63], v[88:91], v[80:83], v[32:47]\n\t" \
"v_exp_f32 v71, v71\n\t" \
"v_exp_f32 v72, v72\n\t" \
"v_exp_f32 v73, v73\n\t" \
"v_smfmac_f32_32x32x32_bf16 v[16:31], v[92:95], v[64:71], v100\n\t" \
"ds_read_b128 v[92:95], v103\n\t" \
"v_exp_f32 v74, v74\n\t" \
"v_exp_f32 v75, v75\n\t" \
"v_exp_f32 v76, v76\n\t" \
"v_exp_f32 v77, v77\n\t" \
"v_exp_f32 v78, v78\n\t" \
"v_exp_f32 v79, v79\n\t" \
"s_setprio 3\n\t" \
"v_exp_f32 v48, v48\n\t" \
"v_exp_f32 v49, v49\n\t" \
"v_smfmac_f32_32x32x32_bf16 v[16:31], v[96:99], v[72:79], v100\n\t" \
"v_exp_f32 v50, v50\n\t" \
"v_exp_f32 v51, v51\n\t" \
"v_add_u32 v102, v107, v103\n\t" \
"v_exp_f32 v52, v52\n\t" \
"v_exp_f32 v53, v53\n\t" \
"v_exp_f32 v54, v54\n\t" \
"v_mfma_f32_32x32x16_bf16 v[64:79], v[88:91], v[84:87], v[32:47]\n\t" \
"ds_read_b128 v[96:99], v103 offset:512\n\t" \
"ds_read_b128 v[88:91], v101 offset:17408\n\t" \
"v_exp_f32 v55, v55\n\t" \
"v_exp_f32 v56, v56\n\t" \
"v_exp_f32 v57, v57\n\t" \
"s_waitcnt lgkmcnt(2)\n\t" \
"v_smfmac_f32_32x32x32_bf16 v[0:15], v[92:95], v[48:55], v100\n\t" \
"v_exp_f32 v58, v58\n\t" \
"v_exp_f32 v59, v59\n\t" \
"s_add_u32 %[wo], %[wo], 0x4000\n\t" \
"v_exp_f32 v60, v60\n\t" \
"buffer_load_dwordx4 v[168:171], v106, %[wr], %[wo] offen\n\t" \
"v_exp_f32 v61, v61\n\t" \
"v_exp_f32 v62, v62\n\t" \
"v_exp_f32 v63, v63\n\t" \
"s_setprio 2\n\t" \
"v_exp_f32 v64, v64\n\t" \
"v_exp_f32 v65, v65\n\t" \
"s_waitcnt lgkmcnt(0)\n\t" \
"v_smfmac_f32_32x32x32_bf16 v[0:15], v[96:99], v[56:63], v100\n\t" \
"v_exp_f32 v66, v66\n\t" \
"s_getpc_b64 s[88:89]\n5:\n\t" \
"v_exp_f32 v67, v67\n\t" \
"s_sub_u32 s88, s88, 5b-.Lfb_begin\n\t" \
"v_exp_f32 v68, v68\n\t" \
"s_subb_u32 s89, s89, 0\n\t" \
"v_exp_f32 v69, v69\n\t" \
"global_load_dword v104, v125, s[88:89]\n\t" \
"v_exp_f32 v70, v70\n\t" \
"v_mfma_f32_32x32x16_bf16 v[48:63], v[88:91], v[80:83], v[32:47]\n\t" \
"v_exp_f32 v71, v71\n\t" \
"v_exp_f32 v72, v72\n\t" \
"v_exp_f32 v73, v73\n\t" \
"v_smfmac_f32_32x32x32_bf16 v[16:31], v[92:95], v[64:71], v100\n\t" \
"ds_read_b128 v[92:95], v103 offset:1024\n\t" \
"v_exp_f32 v74, v74\n\t" \
"v_exp_f32 v75, v75\n\t" \
"s_add_u32 %[wo], %[wo], 0x4000\n\t" \
"v_exp_f32 v76, v76\n\t" \
"buffer_load_dwordx4 v[172:175], v106, %[wr], %[wo] offen\n\t" \
"v_exp_f32 v77, v77\n\t" \
"v_exp_f32 v78, v78\n\t" \
"v_exp_f32 v79, v79\n\t" \
"s_setprio 1\n\t" \
"v_exp_f32 v48, v48\n\t" \
"v_exp_f32 v49, v49\n\t" \
"v_smfmac_f32_32x32x32_bf16 v[16:31], v[96:99], v[72:79], v100\n\t" \
"v_exp_f32 v50, v50\n\t" \
"v_exp_f32 v51, v51\n\t" \
"v_exp_f32 v52, v52\n\t" \
"v_exp_f32 v53, v53\n\t" \
"v_exp_f32 v54, v54\n\t" \
"v_mfma_f32_32x32x16_bf16 v[64:79], v[88:91], v[84:87], v[32:47]\n\t" \
"ds_read_b128 v[96:99], v103 offset:1536\n\t" \
"ds_read_b128 v[88:91], v101 offset:18432\n\t" \
"v_exp_f32 v55, v55\n\t" \
"v_exp_f32 v56, v56\n\t" \
"v_exp_f32 v57, v57\n\t" \
"s_waitcnt lgkmcnt(2)\n\t" \
"v_smfmac_f32_32x32x32_bf16 v[0:15], v[92:95], v[48:55], v100\n\t" \
"v_exp_f32 v58, v58\n\t" \
"v_exp_f32 v59, v59\n\t" \
"s_add_u32 %[wo], %[wo], 0x4000\n\t" \
"v_exp_f32 v60, v60\n\t" \
"buffer_load_dwordx4 v[176:179], v106, %[wr], %[wo] offen\n\t" \
"v_exp_f32 v61, v61\n\t" \
"v_exp_f32 v62, v62\n\t" \
"v_exp_f32 v63, v63\n\t" \
"s_setprio 0\n\t" \
"v_exp_f32 v64, v64\n\t" \
"v_exp_f32 v65, v65\n\t" \
"s_waitcnt lgkmcnt(0)\n\t" \
"v_smfmac_f32_32x32x32_bf16 v[0:15], v[96:99], v[56:63], v100\n\t" \
"v_exp_f32 v66, v66\n\t" \
"v_exp_f32 v67, v67\n\t" \
"v_exp_f32 v68, v68\n\t" \
"v_exp_f32 v69, v69\n\t" \
"v_exp_f32 v70, v70\n\t" \
"v_mfma_f32_32x32x16_bf16 v[48:63], v[88:91], v[80:83], v[32:47]\n\t" \
"v_exp_f32 v71, v71\n\t" \
"v_exp_f32 v72, v72\n\t" \
"v_exp_f32 v73, v73\n\t" \
"v_smfmac_f32_32x32x32_bf16 v[16:31], v[92:95], v[64:71], v100\n\t" \
"ds_read_b128 v[92:95], v103 offset:2048\n\t" \
"v_exp_f32 v74, v74\n\t" \
"v_exp_f32 v75, v75\n\t" \
"v_exp_f32 v76, v76\n\t" \
"v_exp_f32 v77, v77\n\t" \
"v_exp_f32 v78, v78\n\t" \
"v_exp_f32 v79, v79\n\t" \
"s_setprio 3\n\t" \
"v_exp_f32 v48, v48\n\t" \
"v_exp_f32 v49, v49\n\t" \
"v_smfmac_f32_32x32x32_bf16 v[16:31], v[96:99], v[72:79], v100\n\t" \
"v_exp_f32 v50, v50\n\t" \
"v_exp_f32 v51, v51\n\t" \
"v_exp_f32 v52, v52\n\t" \
"v_exp_f32 v53, v53\n\t" \
"v_exp_f32 v54, v54\n\t" \
"v_mfma_f32_32x32x16_bf16 v[64:79], v[88:91], v[84:87], v[32:47]\n\t" \
"ds_read_b128 v[96:99], v103 offset:2560\n\t" \
"ds_read_b128 v[88:91], v101 offset:19456\n\t" \
"v_exp_f32 v55, v55\n\t" \
"v_exp_f32 v56, v56\n\t" \
"v_exp_f32 v57, v57\n\t" \
"s_waitcnt lgkmcnt(2)\n\t" \
"v_smfmac_f32_32x32x32_bf16 v[0:15], v[92:95], v[48:55], v100\n\t" \
"v_exp_f32 v58, v58\n\t" \
"v_exp_f32 v59, v59\n\t" \
"s_add_u32 %[wo], %[wo], 0x4000\n\t" \
"v_exp_f32 v60, v60\n\t" \
"buffer_load_dwordx4 v[180:183], v106, %[wr], %[wo] offen\n\t" \
"v_exp_f32 v61, v61\n\t" \
"v_exp_f32 v62, v62\n\t" \
"v_exp_f32 v63, v63\n\t" \
"s_setprio 2\n\t" \
"v_exp_f32 v64, v64\n\t" \
"v_exp_f32 v65, v65\n\t" \
"s_waitcnt lgkmcnt(0)\n\t" \
"v_smfmac_f32_32x32x32_bf16 v[0:15], v[96:99], v[56:63], v100\n\t" \
"v_exp_f32 v66, v66\n\t" \
"v_exp_f32 v67, v67\n\t" \
"v_exp_f32 v68, v68\n\t" \
"v_exp_f32 v69, v69\n\t" \
"v_exp_f32 v70, v70\n\t" \
"v_mfma_f32_32x32x16_bf16 v[48:63], v[88:91], v[80:83], v[32:47]\n\t" \
"v_exp_f32 v71, v71\n\t" \
"v_exp_f32 v72, v72\n\t" \
"v_exp_f32 v73, v73\n\t" \
"v_smfmac_f32_32x32x32_bf16 v[16:31], v[92:95], v[64:71], v100\n\t" \
"ds_read_b128 v[92:95], v103 offset:3072\n\t" \
"v_exp_f32 v74, v74\n\t" \
"v_exp_f32 v75, v75\n\t" \
"s_add_u32 %[wo], %[wo], 0x4000\n\t" \
"v_exp_f32 v76, v76\n\t" \
"buffer_load_dwordx4 v[184:187], v106, %[wr], %[wo] offen\n\t" \
"v_exp_f32 v77, v77\n\t" \
"v_exp_f32 v78, v78\n\t" \
"v_exp_f32 v79, v79\n\t" \
"s_setprio 1\n\t" \
"v_exp_f32 v48, v48\n\t" \
"v_exp_f32 v49, v49\n\t" \
"v_smfmac_f32_32x32x32_bf16 v[16:31], v[96:99], v[72:79], v100\n\t" \
"v_exp_f32 v50, v50\n\t" \
"v_exp_f32 v51, v51\n\t" \
"v_exp_f32 v52, v52\n\t" \
"v_exp_f32 v53, v53\n\t" \
"v_exp_f32 v54, v54\n\t" \
"v_mfma_f32_32x32x16_bf16 v[64:79], v[88:91], v[84:87], v[32:47]\n\t" \
"ds_read_b128 v[96:99], v103 offset:3584\n\t" \
"ds_read_b128 v[88:91], v101 offset:32768\n\t" \
"v_exp_f32 v55, v55\n\t" \
"v_exp_f32 v56, v56\n\t" \
"v_exp_f32 v57, v57\n\t" \
"s_waitcnt lgkmcnt(2)\n\t" \
"v_smfmac_f32_32x32x32_bf16 v[0:15], v[92:95], v[48:55], v100\n\t" \
"v_exp_f32 v58, v58\n\t" \
"v_exp_f32 v59, v59\n\t" \
"v_exp_f32 v60, v60\n\t" \
"v_exp_f32 v61, v61\n\t" \
"v_exp_f32 v62, v62\n\t" \
"v_exp_f32 v63, v63\n\t" \
"s_setprio 0\n\t" \
"v_exp_f32 v64, v64\n\t" \
"v_exp_f32 v65, v65\n\t" \
"s_waitcnt lgkmcnt(0)\n\t" \
"v_smfmac_f32_32x32x32_bf16 v[0:15], v[96:99], v[56:63], v100\n\t" \
"v_exp_f32 v66, v66\n\t" \
"v_exp_f32 v67, v67\n\t" \
"v_exp_f32 v68, v68\n\t" \
"v_exp_f32 v69, v69\n\t" \
"v_exp_f32 v70, v70\n\t" \
"v_mfma_f32_32x32x16_bf16 v[48:63], v[88:91], v[80:83], v[32:47]\n\t" \
"v_exp_f32 v71, v71\n\t" \
"v_exp_f32 v72, v72\n\t" \
"v_exp_f32 v73, v73\n\t" \
"v_smfmac_f32_32x32x32_bf16 v[16:31], v[92:95], v[64:71], v100\n\t" \
"ds_read_b128 v[92:95], v102\n\t" \
"v_exp_f32 v74, v74\n\t" \
"v_exp_f32 v75, v75\n\t" \
"v_exp_f32 v76, v76\n\t" \
"v_exp_f32 v77, v77\n\t" \
"v_exp_f32 v78, v78\n\t" \
"v_exp_f32 v79, v79\n\t" \
"s_mov_b32 m0, %[keep]\n\t" \
"9:\n\t" \
"s_setprio 0\n\t" \
"s_waitcnt lgkmcnt(0)\n\t" \
"v_smfmac_f32_32x32x32_bf16 v[16:31], v[96:99], v[72:79], v100\n\t" \
"s_waitcnt vmcnt(0)\n\t" \
"s_barrier\n\t" \
"s_nop 11\n\t"

#define AT_PRO \
"s_waitcnt vmcnt(4)\n\t" \
"v_max_f32 v108, v112, v113\n\t" \
"v_dot2_f32_bf16 v109, v80, v80, 0\n\t" \
"v_dot2_f32_bf16 v110, v84, v84, 0\n\t" \
"v_max_f32_dpp v108, v108, v108 quad_perm:[1,0,3,2] row_mask:0xf bank_mask:0xf\n\t" \
"v_dot2_f32_bf16 v109, v81, v81, v109\n\t" \
"v_dot2_f32_bf16 v110, v85, v85, v110\n\t" \
"v_max_f32_dpp v108, v108, v108 quad_perm:[2,3,0,1] row_mask:0xf bank_mask:0xf\n\t" \
"v_dot2_f32_bf16 v109, v82, v82, v109\n\t" \
"v_dot2_f32_bf16 v110, v86, v86, v110\n\t" \
"v_max_f32_dpp v108, v108, v108 row_half_mirror row_mask:0xf bank_mask:0xf\n\t" \
"v_dot2_f32_bf16 v109, v83, v83, v109\n\t" \
"v_dot2_f32_bf16 v110, v87, v87, v110\n\t" \
"v_max_f32_dpp v108, v108, v108 row_mirror row_mask:0xf bank_mask:0xf\n\t" \
"s_nop 1\n\t" \
"v_mov_b32 v111, v108\n\t" \
"s_nop 1\n\t" \
"v_permlane32_swap_b32 v109, v110\n\t" \
"v_permlane16_swap_b32 v108, v111\n\t" \
"v_add_f32 v109, v109, v110\n\t" \
"v_max_f32 v108, v108, v111\n\t" \
"v_mov_b32 v110, v109\n\t" \
"v_mov_b32 v111, v108\n\t" \
"s_nop 1\n\t" \
"v_permlane32_swap_b32 v109, v110\n\t" \
"v_permlane32_swap_b32 v108, v111\n\t" \
"v_max_f32 v109, v109, v110\n\t" \
"v_max_f32 v108, v108, v111\n\t" \
"v_mul_f32 v109, v109, v108\n\t" \
"v_sqrt_f32 v109, v109\n\t" \
"s_nop 0\n\t" \
"v_mul_f32 v32, 0xbf8147ae, v109\n\t" \
"v_cmp_nle_f32 vcc, 0xc2480000, v32\n\t" \
"v_mov_b32 v33, v32\n\t" \
"v_mov_b32 v34, v32\n\t" \
"v_mov_b32 v35, v32\n\t" \
"s_cmp_lg_u64 vcc, 0\n\t" \
"s_cselect_b32 %[slow], 1, 0\n\t" \
"v_mov_b32 v109, %[slow]\n\t" \
"ds_write_b32 v116, v109\n\t" \
"v_mov_b64 v[36:37], v[32:33]\n\t" \
"v_mov_b64 v[38:39], v[32:33]\n\t" \
"v_mov_b64 v[40:41], v[32:33]\n\t" \
"v_mov_b64 v[42:43], v[32:33]\n\t" \
"v_mov_b64 v[44:45], v[32:33]\n\t" \
"v_mov_b64 v[46:47], v[32:33]\n\t" \
"s_waitcnt vmcnt(2) lgkmcnt(0)\n\t" \
"s_barrier\n\t" \
"ds_read_b128 v[88:91], v101\n\t" \
"ds_read_b128 v[108:111], v117\n\t" \
"ds_read_b128 v[112:115], v117 offset:16\n\t" \
"ds_read_b128 v[92:95], v102\n\t" \
"s_waitcnt lgkmcnt(3)\n\t" \
"v_mfma_f32_32x32x16_bf16 v[48:63], v[88:91], v[80:83], v[32:47]\n\t" \
"s_waitcnt lgkmcnt(1)\n\t" \
"v_or3_b32 v108, v108, v109, v110\n\t" \
"v_or3_b32 v111, v111, v112, v113\n\t" \
"v_or3_b32 v108, v108, v114, v115\n\t" \
"v_or_b32 v108, v108, v111\n\t" \
"s_nop 0\n\t" \
"v_readfirstlane_b32 %[slow], v108\n\t" \
"s_waitcnt lgkmcnt(0)\n\t" \
"s_nop 3\n\t"

#define AT_SLOW \
"s_cmp_eq_u32 %[slow], 0\n\t" \
"s_cbranch_scc1 9f\n\t" \
"s_mov_b32 %[keep], m0\n\t" \
"ds_read_b64_tr_b16 v[224:225], v118 offset:0\n\t" \
"ds_read_b64_tr_b16 v[226:227], v118 offset:2048\n\t" \
"ds_read_b64_tr_b16 v[228:229], v118 offset:8192\n\t" \
"ds_read_b64_tr_b16 v[230:231], v118 offset:10240\n\t" \
"ds_read_b64_tr_b16 v[232:233], v118 offset:16384\n\t" \
"ds_read_b64_tr_b16 v[234:235], v118 offset:18432\n\t" \
"ds_read_b64_tr_b16 v[236:237], v118 offset:24576\n\t" \
"ds_read_b64_tr_b16 v[238:239], v118 offset:26624\n\t" \
"s_waitcnt lgkmcnt(0)\n\t" \
"ds_read_b64_tr_b16 v[240:241], v118 offset:32768\n\t" \
"ds_read_b64_tr_b16 v[242:243], v118 offset:34816\n\t" \
"ds_read_b64_tr_b16 v[244:245], v118 offset:40960\n\t" \
"ds_read_b64_tr_b16 v[246:247], v118 offset:43008\n\t" \
"ds_read_b64_tr_b16 v[248:249], v118 offset:49152\n\t" \
"ds_read_b64_tr_b16 v[250:251], v118 offset:51200\n\t" \
"ds_read_b64_tr_b16 v[252:253], v118 offset:57344\n\t" \
"ds_read_b64_tr_b16 v[254:255], v118 offset:59392\n\t" \
"s_waitcnt lgkmcnt(0)\n\t" \
"s_barrier\n\t" \
"s_add_u32 m0, %[kl], 0x8000\n\t" \
"s_mov_b32 %[c], 6\n\t" \
"1:\n\t" \
"global_load_lds_dwordx4 v121, s[88:89] sc1\n\t" \
"s_add_u32 m0, m0, 0x2000\n\t" \
"s_add_u32 s88, s88, 0x1000\n\t" \
"s_addc_u32 s89, s89, 0\n\t" \
"global_load_lds_dwordx4 v121, s[90:91] sc1\n\t" \
"s_add_u32 m0, m0, 0x2000\n\t" \
"s_add_u32 s90, s90, 0x1000\n\t" \
"s_addc_u32 s91, s91, 0\n\t" \
"s_sub_u32 %[c], %[c], 1\n\t" \
"s_cmp_lg_u32 %[c], 0\n\t" \
"s_cbranch_scc1 1b\n\t" \
"v_mov_b32 v119, 0xff61b1e6\n\t" \
"v_mov_b32 v120, 0xff61b1e6\n\t" \
"v_subrev_u32 v103, 0x1000, v107\n\t" \
"s_waitcnt vmcnt(0)\n\t" \
"s_barrier\n\t" \
"2:\n\t" \
"ds_read_b128 v[88:91], v101\n\t" \
"ds_read_b128 v[92:95], v102\n\t" \
"ds_read_b128 v[96:99], v102 offset:512\n\t" \
"v_add_u32 v101, 0x400, v101\n\t" \
"v_add_u32 v102, 0x400, v102\n\t" \
"s_add_u32 %[c], %[c], 1\n\t" \
"s_and_b32 %[t], %[c], 3\n\t" \
"s_cmp_lg_u32 %[t], 0\n\t" \
"s_cbranch_scc1 3f\n\t" \
"v_add_u32 v101, 0x3000, v101\n\t" \
"v_add_u32 v102, v103, v102\n\t" \
"3:\n\t" \
"s_waitcnt lgkmcnt(0)\n\t" \
"v_mfma_f32_32x32x16_bf16 v[48:63], v[88:91], v[80:83], 0\n\t" \
"s_nop 11\n\t" \
"v_max3_f32 v108, v48, v49, v119\n\t" \
"v_max3_f32 v108, v50, v51, v108\n\t" \
"v_max3_f32 v108, v52, v53, v108\n\t" \
"v_max3_f32 v108, v54, v55, v108\n\t" \
"v_max3_f32 v108, v56, v57, v108\n\t" \
"v_max3_f32 v108, v58, v59, v108\n\t" \
"v_max3_f32 v108, v60, v61, v108\n\t" \
"v_max3_f32 v108, v62, v63, v108\n\t" \
"v_mov_b32 v109, v108\n\t" \
"s_nop 1\n\t" \
"v_permlane32_swap_b32 v108, v109\n\t" \
"v_max_f32 v108, v108, v109\n\t" \
"v_sub_f32 v109, v119, v108\n\t" \
"v_mov_b32 v119, v108\n\t" \
"v_exp_f32 v109, v109\n\t" \
"v_sub_f32 v48, v48, v108\n\t" \
"v_sub_f32 v49, v49, v108\n\t" \
"v_sub_f32 v50, v50, v108\n\t" \
"v_sub_f32 v51, v51, v108\n\t" \
"v_sub_f32 v52, v52, v108\n\t" \
"v_sub_f32 v53, v53, v108\n\t" \
"v_sub_f32 v54, v54, v108\n\t" \
"v_sub_f32 v55, v55, v108\n\t" \
"v_sub_f32 v56, v56, v108\n\t" \
"v_sub_f32 v57, v57, v108\n\t" \
"v_sub_f32 v58, v58, v108\n\t" \
"v_sub_f32 v59, v59, v108\n\t" \
"v_sub_f32 v60, v60, v108\n\t" \
"v_sub_f32 v61, v61, v108\n\t" \
"v_sub_f32 v62, v62, v108\n\t" \
"v_sub_f32 v63, v63, v108\n\t" \
"v_exp_f32 v48, v48\n\t" \
"v_exp_f32 v49, v49\n\t" \
"v_exp_f32 v50, v50\n\t" \
"v_exp_f32 v51, v51\n\t" \
"v_exp_f32 v52, v52\n\t" \
"v_exp_f32 v53, v53\n\t" \
"v_exp_f32 v54, v54\n\t" \
"v_exp_f32 v55, v55\n\t" \
"v_exp_f32 v56, v56\n\t" \
"v_exp_f32 v57, v57\n\t" \
"v_exp_f32 v58, v58\n\t" \
"v_exp_f32 v59, v59\n\t" \
"v_exp_f32 v60, v60\n\t" \
"v_exp_f32 v61, v61\n\t" \
"v_exp_f32 v62, v62\n\t" \
"v_exp_f32 v63, v63\n\t" \
"v_mul_f32 v0, v109, v0\n\t" \
"v_mul_f32 v1, v109, v1\n\t" \
"v_mul_f32 v2, v109, v2\n\t" \
"v_mul_f32 v3, v109, v3\n\t" \
"v_mul_f32 v4, v109, v4\n\t" \
"v_mul_f32 v5, v109, v5\n\t" \
"v_mul_f32 v6, v109, v6\n\t" \
"v_mul_f32 v7, v109, v7\n\t" \
"v_mul_f32 v8, v109, v8\n\t" \
"s_nop 1\n\t" \
"v_smfmac_f32_32x32x32_bf16 v[0:15], v[92:95], v[48:55], v100\n\t" \
"v_smfmac_f32_32x32x32_bf16 v[0:15], v[96:99], v[56:63], v100\n\t" \
"v_mfma_f32_32x32x16_bf16 v[48:63], v[88:91], v[84:87], 0\n\t" \
"s_nop 11\n\t" \
"v_max3_f32 v108, v48, v49, v120\n\t" \
"v_max3_f32 v108, v50, v51, v108\n\t" \
"v_max3_f32 v108, v52, v53, v108\n\t" \
"v_max3_f32 v108, v54, v55, v108\n\t" \
"v_max3_f32 v108, v56, v57, v108\n\t" \
"v_max3_f32 v108, v58, v59, v108\n\t" \
"v_max3_f32 v108, v60, v61, v108\n\t" \
"v_max3_f32 v108, v62, v63, v108\n\t" \
"v_mov_b32 v109, v108\n\t" \
"s_nop 1\n\t" \
"v_permlane32_swap_b32 v108, v109\n\t" \
"v_max_f32 v108, v108, v109\n\t" \
"v_sub_f32 v109, v120, v108\n\t" \
"v_mov_b32 v120, v108\n\t" \
"v_exp_f32 v109, v109\n\t" \
"v_sub_f32 v48, v48, v108\n\t" \
"v_sub_f32 v49, v49, v108\n\t" \
"v_sub_f32 v50, v50, v108\n\t" \
"v_sub_f32 v51, v51, v108\n\t" \
"v_sub_f32 v52, v52, v108\n\t" \
"v_sub_f32 v53, v53, v108\n\t" \
"v_sub_f32 v54, v54, v108\n\t" \
"v_sub_f32 v55, v55, v108\n\t" \
"v_sub_f32 v56, v56, v108\n\t" \
"v_sub_f32 v57, v57, v108\n\t" \
"v_sub_f32 v58, v58, v108\n\t" \
"v_sub_f32 v59, v59, v108\n\t" \
"v_sub_f32 v60, v60, v108\n\t" \
"v_sub_f32 v61, v61, v108\n\t" \
"v_sub_f32 v62, v62, v108\n\t" \
"v_sub_f32 v63, v63, v108\n\t" \
"v_exp_f32 v48, v48\n\t" \
"v_exp_f32 v49, v49\n\t" \
"v_exp_f32 v50, v50\n\t" \
"v_exp_f32 v51, v51\n\t" \
"v_exp_f32 v52, v52\n\t" \
"v_exp_f32 v53, v53\n\t" \
"v_exp_f32 v54, v54\n\t" \
"v_exp_f32 v55, v55\n\t" \
"v_exp_f32 v56, v56\n\t" \
"v_exp_f32 v57, v57\n\t" \
"v_exp_f32 v58, v58\n\t" \
"v_exp_f32 v59, v59\n\t" \
"v_exp_f32 v60, v60\n\t" \
"v_exp_f32 v61, v61\n\t" \
"v_exp_f32 v62, v62\n\t" \
"v_exp_f32 v63, v63\n\t" \
"v_mul_f32 v16, v109, v16\n\t" \
"v_mul_f32 v17, v109, v17\n\t" \
"v_mul_f32 v18, v109, v18\n\t" \
"v_mul_f32 v19, v109, v19\n\t" \
"v_mul_f32 v20, v109, v20\n\t" \
"v_mul_f32 v21, v109, v21\n\t" \
"v_mul_f32 v22, v109, v22\n\t" \
"v_mul_f32 v23, v109, v23\n\t" \
"v_mul_f32 v24, v109, v24\n\t" \
"s_nop 1\n\t" \
"v_smfmac_f32_32x32x32_bf16 v[16:31], v[92:95], v[48:55], v100\n\t" \
"v_smfmac_f32_32x32x32_bf16 v[16:31], v[96:99], v[56:63], v100\n\t" \
"s_cmp_lg_u32 %[c], 32\n\t" \
"s_cbranch_scc1 2b\n\t" \
"v_mov_b64 v[96:97], 0\n\t" \
"v_mov_b64 v[98:99], 0\n\t" \
"s_barrier\n\t" \
"s_add_u32 m0, %[gl], 0\n\t" \
"s_nop 0\n\t" \
"global_load_lds_dwordx4 v122, %[g0]\n\t" \
"s_add_u32 m0, %[gl], 1024\n\t" \
"s_nop 0\n\t" \
"global_load_lds_dwordx4 v123, %[g0]\n\t" \
"s_add_u32 m0, %[gl], 2048\n\t" \
"s_nop 0\n\t" \
"global_load_lds_dwordx4 v124, %[g0]\n\t" \
"s_add_u32 m0, %[gl], 3072\n\t" \
"s_nop 0\n\t" \
"global_load_lds_dwordx4 v122, %[g1]\n\t" \
"s_add_u32 m0, %[gl], 4096\n\t" \
"s_nop 0\n\t" \
"global_load_lds_dwordx4 v123, %[g1]\n\t" \
"s_add_u32 m0, %[gl], 5120\n\t" \
"s_nop 0\n\t" \
"global_load_lds_dwordx4 v124, %[g1]\n\t" \
"buffer_load_dwordx4 v[128:131], v106, %[wr], %[wo] offen\n\t" \
"s_add_u32 %[wo], %[wo], 0x4000\n\t" \
"buffer_load_dwordx4 v[132:135], v106, %[wr], %[wo] offen\n\t" \
"s_add_u32 %[wo], %[wo], 0x4000\n\t" \
"buffer_load_dwordx4 v[136:139], v106, %[wr], %[wo] offen\n\t" \
"s_add_u32 %[wo], %[wo], 0x4000\n\t" \
"buffer_load_dwordx4 v[140:143], v106, %[wr], %[wo] offen\n\t" \
"s_add_u32 %[wo], %[wo], 0x4000\n\t" \
"buffer_load_dwordx4 v[144:147], v106, %[wr], %[wo] offen\n\t" \
"s_add_u32 %[wo], %[wo], 0x4000\n\t" \
"buffer_load_dwordx4 v[148:151], v106, %[wr], %[wo] offen\n\t" \
"s_add_u32 %[wo], %[wo], 0x4000\n\t" \
"buffer_load_dwordx4 v[152:155], v106, %[wr], %[wo] offen\n\t" \
"s_add_u32 %[wo], %[wo], 0x4000\n\t" \
"buffer_load_dwordx4 v[156:159], v106, %[wr], %[wo] offen\n\t" \
"s_add_u32 %[wo], %[wo], 0x4000\n\t" \
"buffer_load_dwordx4 v[160:163], v106, %[wr], %[wo] offen\n\t" \
"s_add_u32 %[wo], %[wo], 0x4000\n\t" \
"buffer_load_dwordx4 v[164:167], v106, %[wr], %[wo] offen\n\t" \
"s_add_u32 %[wo], %[wo], 0x4000\n\t" \
"buffer_load_dwordx4 v[168:171], v106, %[wr], %[wo] offen\n\t" \
"s_add_u32 %[wo], %[wo], 0x4000\n\t" \
"buffer_load_dwordx4 v[172:175], v106, %[wr], %[wo] offen\n\t" \
"s_add_u32 %[wo], %[wo], 0x4000\n\t" \
"buffer_load_dwordx4 v[176:179], v106, %[wr], %[wo] offen\n\t" \
"s_add_u32 %[wo], %[wo], 0x4000\n\t" \
"buffer_load_dwordx4 v[180:183], v106, %[wr], %[wo] offen\n\t" \
"s_add_u32 %[wo], %[wo], 0x4000\n\t" \
"buffer_load_dwordx4 v[184:187], v106, %[wr], %[wo] offen\n\t" \
"global_load_dwordx2 v[188:189], v126, %[x0]\n\t" \
"global_load_dwordx2 v[190:191], v126, %[x1]\n\t" \
"s_mov_b32 m0, %[keep]\n\t" \
"9:\n\t"

#define AT_COMB \
"s_cmp_lg_u32 %[kh], 0\n\t" \
"s_cbranch_scc1 1f\n\t" \
"v_mov_b32 v25, v120\n\t" \
"ds_write_b128 v108, v[16:19]\n\t" \
"ds_write_b128 v108, v[20:23] offset:1024\n\t" \
"ds_write_b64 v108, v[24:25] offset:2048\n\t" \
"s_branch 2f\n\t" \
"1:\n\t" \
"v_mov_b32 v9, v119\n\t" \
"ds_write_b128 v108, v[0:3]\n\t" \
"ds_write_b128 v108, v[4:7] offset:1024\n\t" \
"ds_write_b64 v108, v[8:9] offset:2048\n\t" \
"v_mov_b32 v119, v120\n\t" \
"s_waitcnt lgkmcnt(0)\n\t" \
"v_mov_b64 v[0:1], v[16:17]\n\t" \
"v_mov_b64 v[2:3], v[18:19]\n\t" \
"v_mov_b64 v[4:5], v[20:21]\n\t" \
"v_mov_b64 v[6:7], v[22:23]\n\t" \
"v_mov_b32 v8, v24\n\t" \
"2:\n\t" \
"s_waitcnt lgkmcnt(0)\n\t" \
"s_barrier\n\t" \
"ds_read_b64 v[40:41], v109 offset:2048\n\t" \
"ds_read_b128 v[32:35], v109\n\t" \
"ds_read_b128 v[36:39], v109 offset:1024\n\t" \
"s_waitcnt lgkmcnt(2)\n\t" \
"v_max_f32 v42, v119, v41\n\t" \
"v_sub_f32 v43, v119, v42\n\t" \
"v_sub_f32 v44, v41, v42\n\t" \
"v_exp_f32 v43, v43\n\t" \
"v_exp_f32 v44, v44\n\t" \
"v_mul_f32 v45, v8, v43\n\t" \
"v_fmac_f32 v45, v40, v44\n\t" \
"v_rcp_f32 v45, v45\n\t" \
"s_nop 0\n\t" \
"v_mul_f32 v43, v45, v43\n\t" \
"v_mul_f32 v44, v45, v44\n\t" \
"v_mul_f32 v0, v43, v0\n\t" \
"v_mul_f32 v1, v43, v1\n\t" \
"v_mul_f32 v2, v43, v2\n\t" \
"v_mul_f32 v3, v43, v3\n\t" \
"v_mul_f32 v4, v43, v4\n\t" \
"v_mul_f32 v5, v43, v5\n\t" \
"v_mul_f32 v6, v43, v6\n\t" \
"v_mul_f32 v7, v43, v7\n\t" \
"s_waitcnt lgkmcnt(0)\n\t" \
"v_fmac_f32 v0, v44, v32\n\t" \
"v_fmac_f32 v1, v44, v33\n\t" \
"v_fmac_f32 v2, v44, v34\n\t" \
"v_fmac_f32 v3, v44, v35\n\t" \
"v_fmac_f32 v4, v44, v36\n\t" \
"v_fmac_f32 v5, v44, v37\n\t" \
"v_fmac_f32 v6, v44, v38\n\t" \
"v_fmac_f32 v7, v44, v39\n\t" \
"v_cvt_pk_bf16_f32 v0, v0, v1\n\t" \
"v_cvt_pk_bf16_f32 v1, v2, v3\n\t" \
"v_cvt_pk_bf16_f32 v2, v4, v5\n\t" \
"v_cvt_pk_bf16_f32 v3, v6, v7\n\t" \
"buffer_store_dwordx4 v[0:3], v121, %[rs], %[oo] offen sc1\n\t" \
"s_waitcnt vmcnt(0)\n\t" \
"s_barrier\n\t"

#define P3_BODY \
"s_waitcnt vmcnt(2)\n\t" \
"s_barrier\n\t" \
"ds_read_b128 v[8:11], v104 offset:0\n\t" \
"ds_read_b128 v[12:15], v104 offset:256\n\t" \
"ds_read_b128 v[16:19], v104 offset:2048\n\t" \
"ds_read_b128 v[20:23], v104 offset:2304\n\t" \
"ds_read_b128 v[24:27], v104 offset:4096\n\t" \
"ds_read_b128 v[28:31], v104 offset:4352\n\t" \
"ds_read_b128 v[32:35], v104 offset:6144\n\t" \
"ds_read_b128 v[36:39], v104 offset:6400\n\t" \
"ds_read_b128 v[40:43], v104 offset:8192\n\t" \
"ds_read_b128 v[44:47], v104 offset:8448\n\t" \
"ds_read_b128 v[48:51], v104 offset:10240\n\t" \
"ds_read_b128 v[52:55], v104 offset:10496\n\t" \
"ds_read_b128 v[56:59], v104 offset:12288\n\t" \
"ds_read_b128 v[60:63], v104 offset:12544\n\t" \
"s_waitcnt lgkmcnt(13)\n\t" \
"v_mfma_f32_16x16x32_bf16 v[0:3], v[192:195], v[8:11], v[0:3]\n\t" \
"ds_read_b128 v[64:67], v104 offset:14336\n\t" \
"s_waitcnt lgkmcnt(13)\n\t" \
"v_mfma_f32_16x16x32_bf16 v[4:7], v[192:195], v[12:15], v[4:7]\n\t" \
"ds_read_b128 v[68:71], v104 offset:14592\n\t" \
"s_waitcnt lgkmcnt(13)\n\t" \
"v_mfma_f32_16x16x32_bf16 v[0:3], v[196:199], v[16:19], v[0:3]\n\t" \
"s_waitcnt lgkmcnt(12)\n\t" \
"v_mfma_f32_16x16x32_bf16 v[4:7], v[196:199], v[20:23], v[4:7]\n\t" \
"s_waitcnt lgkmcnt(11)\n\t" \
"v_mfma_f32_16x16x32_bf16 v[0:3], v[200:203], v[24:27], v[0:3]\n\t" \
"s_waitcnt lgkmcnt(10)\n\t" \
"v_mfma_f32_16x16x32_bf16 v[4:7], v[200:203], v[28:31], v[4:7]\n\t" \
"s_waitcnt lgkmcnt(9)\n\t" \
"v_mfma_f32_16x16x32_bf16 v[0:3], v[204:207], v[32:35], v[0:3]\n\t" \
"s_waitcnt lgkmcnt(8)\n\t" \
"v_mfma_f32_16x16x32_bf16 v[4:7], v[204:207], v[36:39], v[4:7]\n\t" \
"s_waitcnt vmcnt(0) lgkmcnt(0)\n\t" \
"s_barrier\n\t" \
"ds_read_b128 v[72:75], v104 offset:16384\n\t" \
"ds_read_b128 v[76:79], v104 offset:16640\n\t" \
"ds_read_b128 v[80:83], v104 offset:18432\n\t" \
"ds_read_b128 v[84:87], v104 offset:18688\n\t" \
"ds_read_b128 v[88:91], v104 offset:20480\n\t" \
"ds_read_b128 v[92:95], v104 offset:20736\n\t" \
"ds_read_b128 v[96:99], v104 offset:22528\n\t" \
"v_mfma_f32_16x16x32_bf16 v[0:3], v[208:211], v[40:43], v[0:3]\n\t" \
"ds_read_b128 v[100:103], v104 offset:22784\n\t" \
"v_mfma_f32_16x16x32_bf16 v[4:7], v[208:211], v[44:47], v[4:7]\n\t" \
"ds_read_b128 v[8:11], v104 offset:24576\n\t" \
"v_mfma_f32_16x16x32_bf16 v[0:3], v[212:215], v[48:51], v[0:3]\n\t" \
"ds_read_b128 v[12:15], v104 offset:24832\n\t" \
"v_mfma_f32_16x16x32_bf16 v[4:7], v[212:215], v[52:55], v[4:7]\n\t" \
"ds_read_b128 v[16:19], v104 offset:26624\n\t" \
"v_mfma_f32_16x16x32_bf16 v[0:3], v[216:219], v[56:59], v[0:3]\n\t" \
"ds_read_b128 v[20:23], v104 offset:26880\n\t" \
"v_mfma_f32_16x16x32_bf16 v[4:7], v[216:219], v[60:63], v[4:7]\n\t" \
"ds_read_b128 v[24:27], v104 offset:28672\n\t" \
"v_mfma_f32_16x16x32_bf16 v[0:3], v[220:223], v[64:67], v[0:3]\n\t" \
"ds_read_b128 v[28:31], v104 offset:28928\n\t" \
"v_mfma_f32_16x16x32_bf16 v[4:7], v[220:223], v[68:71], v[4:7]\n\t" \
"ds_read_b128 v[32:35], v104 offset:30720\n\t" \
"s_waitcnt lgkmcnt(14)\n\t" \
"ds_read_b128 v[36:39], v104 offset:30976\n\t" \
"s_waitcnt lgkmcnt(15)\n\t" \
"v_mfma_f32_16x16x32_bf16 v[0:3], v[224:227], v[72:75], v[0:3]\n\t" \
"s_waitcnt lgkmcnt(14)\n\t" \
"v_mfma_f32_16x16x32_bf16 v[4:7], v[224:227], v[76:79], v[4:7]\n\t" \
"s_waitcnt lgkmcnt(13)\n\t" \
"v_mfma_f32_16x16x32_bf16 v[0:3], v[228:231], v[80:83], v[0:3]\n\t" \
"s_waitcnt lgkmcnt(12)\n\t" \
"v_mfma_f32_16x16x32_bf16 v[4:7], v[228:231], v[84:87], v[4:7]\n\t" \
"s_waitcnt lgkmcnt(11)\n\t" \
"v_mfma_f32_16x16x32_bf16 v[0:3], v[232:235], v[88:91], v[0:3]\n\t" \
"s_waitcnt lgkmcnt(10)\n\t" \
"v_mfma_f32_16x16x32_bf16 v[4:7], v[232:235], v[92:95], v[4:7]\n\t" \
"s_waitcnt lgkmcnt(9)\n\t" \
"v_mfma_f32_16x16x32_bf16 v[0:3], v[236:239], v[96:99], v[0:3]\n\t" \
"s_waitcnt lgkmcnt(8)\n\t" \
"v_mfma_f32_16x16x32_bf16 v[4:7], v[236:239], v[100:103], v[4:7]\n\t" \
"s_waitcnt lgkmcnt(7)\n\t" \
"v_mfma_f32_16x16x32_bf16 v[0:3], v[240:243], v[8:11], v[0:3]\n\t" \
"s_waitcnt lgkmcnt(6)\n\t" \
"v_mfma_f32_16x16x32_bf16 v[4:7], v[240:243], v[12:15], v[4:7]\n\t" \
"s_waitcnt lgkmcnt(5)\n\t" \
"v_mfma_f32_16x16x32_bf16 v[0:3], v[244:247], v[16:19], v[0:3]\n\t" \
"s_waitcnt lgkmcnt(4)\n\t" \
"v_mfma_f32_16x16x32_bf16 v[4:7], v[244:247], v[20:23], v[4:7]\n\t" \
"s_waitcnt lgkmcnt(3)\n\t" \
"v_mfma_f32_16x16x32_bf16 v[0:3], v[248:251], v[24:27], v[0:3]\n\t" \
"s_waitcnt lgkmcnt(2)\n\t" \
"v_mfma_f32_16x16x32_bf16 v[4:7], v[248:251], v[28:31], v[4:7]\n\t" \
"s_waitcnt lgkmcnt(1)\n\t" \
"v_mfma_f32_16x16x32_bf16 v[0:3], v[252:255], v[32:35], v[0:3]\n\t" \
"s_waitcnt lgkmcnt(0)\n\t" \
"v_mfma_f32_16x16x32_bf16 v[4:7], v[252:255], v[36:39], v[4:7]\n\t" \
"s_nop 7\n\t" \
"v_cvt_pk_bf16_f32 v8, v0, v1\n\t" \
"v_cvt_pk_bf16_f32 v9, v2, v3\n\t" \
"v_cvt_pk_bf16_f32 v10, v4, v5\n\t" \
"v_cvt_pk_bf16_f32 v11, v6, v7\n\t" \
"v_add_f32 v12, v0, v1\n\t" \
"v_add_f32 v13, v4, v5\n\t" \
"v_permlane32_swap_b32 v8, v10\n\t" \
"v_permlane32_swap_b32 v9, v11\n\t" \
"buffer_store_dwordx4 v[8:11], v105, %[rs], %[ho] offen sc1\n\t" \
"v_mul_f32 v14, v0, v0\n\t" \
"v_mul_f32 v15, v4, v4\n\t" \
"v_add_f32 v12, v12, v2\n\t" \
"v_add_f32 v13, v13, v6\n\t" \
"v_fmac_f32 v14, v1, v1\n\t" \
"v_fmac_f32 v15, v5, v5\n\t" \
"v_add_f32 v12, v12, v3\n\t" \
"v_add_f32 v13, v13, v7\n\t" \
"v_fmac_f32 v14, v2, v2\n\t" \
"v_fmac_f32 v15, v6, v6\n\t" \
"v_fmac_f32 v14, v3, v3\n\t" \
"v_fmac_f32 v15, v7, v7\n\t" \
"s_nop 0\n\t" \
"v_permlane32_swap_b32 v12, v13\n\t" \
"v_permlane32_swap_b32 v14, v15\n\t" \
"v_add_f32 v12, v12, v13\n\t" \
"v_add_f32 v14, v14, v15\n\t" \
"s_nop 1\n\t" \
"v_permlane16_swap_b32 v12, v14\n\t" \
"v_add_f32 v12, v12, v14\n\t" \
"ds_write_b32 v106, v12\n\t" \
"s_waitcnt lgkmcnt(0)\n\t" \
"s_barrier\n\t"

#define WD_WRITE \
"ds_write_b128 v48, v[128:131] offset:0\n\t" \
"ds_write_b128 v48, v[132:135] offset:8192\n\t" \
"ds_write_b128 v48, v[136:139] offset:16384\n\t" \
"ds_write_b128 v48, v[140:143] offset:24576\n\t" \
"ds_write_b128 v48, v[144:147] offset:32768\n\t" \
"ds_write_b128 v48, v[148:151] offset:40960\n\t" \
"ds_write_b128 v48, v[152:155] offset:49152\n\t" \
"ds_write_b128 v48, v[156:159] offset:57344\n\t" \
"v_add_u32 v48, 0x10000, v48\n\t" \
"ds_write_b128 v48, v[160:163] offset:0\n\t" \
"ds_write_b128 v48, v[164:167] offset:8192\n\t" \
"ds_write_b128 v48, v[168:171] offset:16384\n\t" \
"ds_write_b128 v48, v[172:175] offset:24576\n\t" \
"ds_write_b128 v48, v[176:179] offset:32768\n\t" \
"v_add_u32 v48, 0x14000, v48\n\t" \
"ds_write_b128 v48, v[180:183] offset:0\n\t" \
"ds_write_b128 v48, v[184:187] offset:8192\n\t"

#define P4_LOOP \
"s_mov_b32 vcc_lo, %[three]\n\t" \
"s_mov_b32 vcc_hi, 0\n\t" \
"buffer_load_dwordx4 v[188:191], v74, %[rs], %[so] offen offset:0 sc1\n\t" \
"buffer_load_dwordx4 v[192:195], v74, %[rs], %[so] offen offset:1024 sc1\n\t" \
"buffer_load_dwordx4 v[196:199], v74, %[rs], %[so] offen offset:2048 sc1\n\t" \
"ds_read_b64_tr_b16 v[48:49], v72 offset:0\n\t" \
"ds_read_b64_tr_b16 v[50:51], v72 offset:768\n\t" \
"ds_read_b64_tr_b16 v[52:53], v72 offset:32\n\t" \
"ds_read_b64_tr_b16 v[54:55], v72 offset:800\n\t" \
"ds_read_b64_tr_b16 v[56:57], v72 offset:64\n\t" \
"ds_read_b64_tr_b16 v[58:59], v72 offset:832\n\t" \
"ds_read_b64_tr_b16 v[60:61], v72 offset:1536\n\t" \
"ds_read_b64_tr_b16 v[62:63], v72 offset:2304\n\t" \
"ds_read_b64_tr_b16 v[64:65], v72 offset:1568\n\t" \
"ds_read_b64_tr_b16 v[66:67], v72 offset:2336\n\t" \
"ds_read_b64_tr_b16 v[68:69], v72 offset:1600\n\t" \
"ds_read_b64_tr_b16 v[70:71], v72 offset:2368\n\t" \
"s_waitcnt vmcnt(2) lgkmcnt(10)\n\t" \
"v_mfma_f32_32x32x16_bf16 v[0:15], v[48:51], v[188:191], 0\n\t" \
"buffer_load_dwordx4 v[200:203], v74, %[rs], %[so] offen offset:3072 sc1\n\t" \
"s_waitcnt lgkmcnt(8)\n\t" \
"v_mfma_f32_32x32x16_bf16 v[16:31], v[52:55], v[188:191], 0\n\t" \
"s_add_u32 %[so], %[so], 0x1000\n\t" \
"buffer_load_dwordx4 v[204:207], v74, %[rs], %[so] offen offset:0 sc1\n\t" \
"s_waitcnt lgkmcnt(6)\n\t" \
"s_cbranch_vccz 9f\n\t" \
"v_mfma_f32_32x32x16_bf16 v[32:47], v[56:59], v[188:191], 0\n\t" \
"9:\n\t" \
"ds_read_b64_tr_b16 v[48:49], v72 offset:3072\n\t" \
"ds_read_b64_tr_b16 v[50:51], v72 offset:3840\n\t" \
"ds_read_b64_tr_b16 v[52:53], v72 offset:3104\n\t" \
"ds_read_b64_tr_b16 v[54:55], v72 offset:3872\n\t" \
"ds_read_b64_tr_b16 v[56:57], v72 offset:3136\n\t" \
"ds_read_b64_tr_b16 v[58:59], v72 offset:3904\n\t" \
"s_waitcnt vmcnt(3) lgkmcnt(10)\n\t" \
"v_mfma_f32_32x32x16_bf16 v[0:15], v[60:63], v[192:195], v[0:15]\n\t" \
"buffer_load_dwordx4 v[208:211], v74, %[rs], %[so] offen offset:1024 sc1\n\t" \
"s_waitcnt lgkmcnt(8)\n\t" \
"v_mfma_f32_32x32x16_bf16 v[16:31], v[64:67], v[192:195], v[16:31]\n\t" \
"buffer_load_dwordx4 v[212:215], v74, %[rs], %[so] offen offset:2048 sc1\n\t" \
"s_waitcnt lgkmcnt(6)\n\t" \
"s_cbranch_vccz 9f\n\t" \
"v_mfma_f32_32x32x16_bf16 v[32:47], v[68:71], v[192:195], v[32:47]\n\t" \
"9:\n\t" \
"ds_read_b64_tr_b16 v[60:61], v72 offset:4608\n\t" \
"ds_read_b64_tr_b16 v[62:63], v72 offset:5376\n\t" \
"ds_read_b64_tr_b16 v[64:65], v72 offset:4640\n\t" \
"ds_read_b64_tr_b16 v[66:67], v72 offset:5408\n\t" \
"ds_read_b64_tr_b16 v[68:69], v72 offset:4672\n\t" \
"ds_read_b64_tr_b16 v[70:71], v72 offset:5440\n\t" \
"s_waitcnt vmcnt(4) lgkmcnt(10)\n\t" \
"v_mfma_f32_32x32x16_bf16 v[0:15], v[48:51], v[196:199], v[0:15]\n\t" \
"buffer_load_dwordx4 v[216:219], v74, %[rs], %[so] offen offset:3072 sc1\n\t" \
"s_waitcnt lgkmcnt(8)\n\t" \
"v_mfma_f32_32x32x16_bf16 v[16:31], v[52:55], v[196:199], v[16:31]\n\t" \
"s_waitcnt lgkmcnt(6)\n\t" \
"s_cbranch_vccz 9f\n\t" \
"v_mfma_f32_32x32x16_bf16 v[32:47], v[56:59], v[196:199], v[32:47]\n\t" \
"9:\n\t" \
"ds_read_b64_tr_b16 v[48:49], v72 offset:6144\n\t" \
"ds_read_b64_tr_b16 v[50:51], v72 offset:6912\n\t" \
"ds_read_b64_tr_b16 v[52:53], v72 offset:6176\n\t" \
"ds_read_b64_tr_b16 v[54:55], v72 offset:6944\n\t" \
"ds_read_b64_tr_b16 v[56:57], v72 offset:6208\n\t" \
"ds_read_b64_tr_b16 v[58:59], v72 offset:6976\n\t" \
"s_waitcnt vmcnt(4) lgkmcnt(10)\n\t" \
"v_mfma_f32_32x32x16_bf16 v[0:15], v[60:63], v[200:203], v[0:15]\n\t" \
"s_add_u32 %[so], %[so], 0x1000\n\t" \
"buffer_load_dwordx4 v[220:223], v74, %[rs], %[so] offen offset:0 sc1\n\t" \
"s_waitcnt lgkmcnt(8)\n\t" \
"v_mfma_f32_32x32x16_bf16 v[16:31], v[64:67], v[200:203], v[16:31]\n\t" \
"s_waitcnt lgkmcnt(6)\n\t" \
"s_cbranch_vccz 9f\n\t" \
"v_mfma_f32_32x32x16_bf16 v[32:47], v[68:71], v[200:203], v[32:47]\n\t" \
"9:\n\t" \
"ds_read_b64_tr_b16 v[60:61], v72 offset:7680\n\t" \
"ds_read_b64_tr_b16 v[62:63], v72 offset:8448\n\t" \
"ds_read_b64_tr_b16 v[64:65], v72 offset:7712\n\t" \
"ds_read_b64_tr_b16 v[66:67], v72 offset:8480\n\t" \
"ds_read_b64_tr_b16 v[68:69], v72 offset:7744\n\t" \
"ds_read_b64_tr_b16 v[70:71], v72 offset:8512\n\t" \
"s_waitcnt vmcnt(4) lgkmcnt(10)\n\t" \
"v_mfma_f32_32x32x16_bf16 v[0:15], v[48:51], v[204:207], v[0:15]\n\t" \
"buffer_load_dwordx4 v[224:227], v74, %[rs], %[so] offen offset:1024 sc1\n\t" \
"s_waitcnt lgkmcnt(8)\n\t" \
"v_mfma_f32_32x32x16_bf16 v[16:31], v[52:55], v[204:207], v[16:31]\n\t" \
"s_waitcnt lgkmcnt(6)\n\t" \
"s_cbranch_vccz 9f\n\t" \
"v_mfma_f32_32x32x16_bf16 v[32:47], v[56:59], v[204:207], v[32:47]\n\t" \
"9:\n\t" \
"ds_read_b64_tr_b16 v[48:49], v72 offset:9216\n\t" \
"ds_read_b64_tr_b16 v[50:51], v72 offset:9984\n\t" \
"ds_read_b64_tr_b16 v[52:53], v72 offset:9248\n\t" \
"ds_read_b64_tr_b16 v[54:55], v72 offset:10016\n\t" \
"ds_read_b64_tr_b16 v[56:57], v72 offset:9280\n\t" \
"ds_read_b64_tr_b16 v[58:59], v72 offset:10048\n\t" \
"s_waitcnt vmcnt(4) lgkmcnt(10)\n\t" \
"v_mfma_f32_32x32x16_bf16 v[0:15], v[60:63], v[208:211], v[0:15]\n\t" \
"buffer_load_dwordx4 v[228:231], v74, %[rs], %[so] offen offset:2048 sc1\n\t" \
"s_waitcnt lgkmcnt(8)\n\t" \
"v_mfma_f32_32x32x16_bf16 v[16:31], v[64:67], v[208:211], v[16:31]\n\t" \
"s_waitcnt lgkmcnt(6)\n\t" \
"s_cbranch_vccz 9f\n\t" \
"v_mfma_f32_32x32x16_bf16 v[32:47], v[68:71], v[208:211], v[32:47]\n\t" \
"9:\n\t" \
"ds_read_b64_tr_b16 v[60:61], v72 offset:10752\n\t" \
"ds_read_b64_tr_b16 v[62:63], v72 offset:11520\n\t" \
"ds_read_b64_tr_b16 v[64:65], v72 offset:10784\n\t" \
"ds_read_b64_tr_b16 v[66:67], v72 offset:11552\n\t" \
"ds_read_b64_tr_b16 v[68:69], v72 offset:10816\n\t" \
"ds_read_b64_tr_b16 v[70:71], v72 offset:11584\n\t" \
"s_waitcnt vmcnt(4) lgkmcnt(10)\n\t" \
"v_mfma_f32_32x32x16_bf16 v[0:15], v[48:51], v[212:215], v[0:15]\n\t" \
"buffer_load_dwordx4 v[232:235], v74, %[rs], %[so] offen offset:3072 sc1\n\t" \
"s_waitcnt lgkmcnt(8)\n\t" \
"v_mfma_f32_32x32x16_bf16 v[16:31], v[52:55], v[212:215], v[16:31]\n\t" \
"s_waitcnt lgkmcnt(6)\n\t" \
"s_cbranch_vccz 9f\n\t" \
"v_mfma_f32_32x32x16_bf16 v[32:47], v[56:59], v[212:215], v[32:47]\n\t" \
"9:\n\t" \
"ds_read_b64_tr_b16 v[48:49], v72 offset:12288\n\t" \
"ds_read_b64_tr_b16 v[50:51], v72 offset:13056\n\t" \
"ds_read_b64_tr_b16 v[52:53], v72 offset:12320\n\t" \
"ds_read_b64_tr_b16 v[54:55], v72 offset:13088\n\t" \
"ds_read_b64_tr_b16 v[56:57], v72 offset:12352\n\t" \
"ds_read_b64_tr_b16 v[58:59], v72 offset:13120\n\t" \
"s_waitcnt vmcnt(4) lgkmcnt(10)\n\t" \
"v_mfma_f32_32x32x16_bf16 v[0:15], v[60:63], v[216:219], v[0:15]\n\t" \
"s_add_u32 %[so], %[so], 0x1000\n\t" \
"buffer_load_dwordx4 v[236:239], v74, %[rs], %[so] offen offset:0 sc1\n\t" \
"s_waitcnt lgkmcnt(8)\n\t" \
"v_mfma_f32_32x32x16_bf16 v[16:31], v[64:67], v[216:219], v[16:31]\n\t" \
"s_waitcnt lgkmcnt(6)\n\t" \
"s_cbranch_vccz 9f\n\t" \
"v_mfma_f32_32x32x16_bf16 v[32:47], v[68:71], v[216:219], v[32:47]\n\t" \
"9:\n\t" \
"ds_read_b64_tr_b16 v[60:61], v72 offset:13824\n\t" \
"ds_read_b64_tr_b16 v[62:63], v72 offset:14592\n\t" \
"ds_read_b64_tr_b16 v[64:65], v72 offset:13856\n\t" \
"ds_read_b64_tr_b16 v[66:67], v72 offset:14624\n\t" \
"ds_read_b64_tr_b16 v[68:69], v72 offset:13888\n\t" \
"ds_read_b64_tr_b16 v[70:71], v72 offset:14656\n\t" \
"s_waitcnt vmcnt(4) lgkmcnt(10)\n\t" \
"v_mfma_f32_32x32x16_bf16 v[0:15], v[48:51], v[220:223], v[0:15]\n\t" \
"buffer_load_dwordx4 v[240:243], v74, %[rs], %[so] offen offset:1024 sc1\n\t" \
"s_waitcnt lgkmcnt(8)\n\t" \
"v_mfma_f32_32x32x16_bf16 v[16:31], v[52:55], v[220:223], v[16:31]\n\t" \
"s_waitcnt lgkmcnt(6)\n\t" \
"s_cbranch_vccz 9f\n\t" \
"v_mfma_f32_32x32x16_bf16 v[32:47], v[56:59], v[220:223], v[32:47]\n\t" \
"9:\n\t" \
"ds_read_b64_tr_b16 v[48:49], v72 offset:15360\n\t" \
"ds_read_b64_tr_b16 v[50:51], v72 offset:16128\n\t" \
"ds_read_b64_tr_b16 v[52:53], v72 offset:15392\n\t" \
"ds_read_b64_tr_b16 v[54:55], v72 offset:16160\n\t" \
"ds_read_b64_tr_b16 v[56:57], v72 offset:15424\n\t" \
"ds_read_b64_tr_b16 v[58:59], v72 offset:16192\n\t" \
"s_waitcnt vmcnt(4) lgkmcnt(10)\n\t" \
"v_mfma_f32_32x32x16_bf16 v[0:15], v[60:63], v[224:227], v[0:15]\n\t" \
"buffer_load_dwordx4 v[188:191], v74, %[rs], %[so] offen offset:2048 sc1\n\t" \
"s_waitcnt lgkmcnt(8)\n\t" \
"v_mfma_f32_32x32x16_bf16 v[16:31], v[64:67], v[224:227], v[16:31]\n\t" \
"s_waitcnt lgkmcnt(6)\n\t" \
"s_cbranch_vccz 9f\n\t" \
"v_mfma_f32_32x32x16_bf16 v[32:47], v[68:71], v[224:227], v[32:47]\n\t" \
"9:\n\t" \
"ds_read_b64_tr_b16 v[60:61], v72 offset:16896\n\t" \
"ds_read_b64_tr_b16 v[62:63], v72 offset:17664\n\t" \
"ds_read_b64_tr_b16 v[64:65], v72 offset:16928\n\t" \
"ds_read_b64_tr_b16 v[66:67], v72 offset:17696\n\t" \
"ds_read_b64_tr_b16 v[68:69], v72 offset:16960\n\t" \
"ds_read_b64_tr_b16 v[70:71], v72 offset:17728\n\t" \
"s_waitcnt vmcnt(4) lgkmcnt(10)\n\t" \
"v_mfma_f32_32x32x16_bf16 v[0:15], v[48:51], v[228:231], v[0:15]\n\t" \
"buffer_load_dwordx4 v[192:195], v74, %[rs], %[so] offen offset:3072 sc1\n\t" \
"s_waitcnt lgkmcnt(8)\n\t" \
"v_mfma_f32_32x32x16_bf16 v[16:31], v[52:55], v[228:231], v[16:31]\n\t" \
"s_waitcnt lgkmcnt(6)\n\t" \
"s_cbranch_vccz 9f\n\t" \
"v_mfma_f32_32x32x16_bf16 v[32:47], v[56:59], v[228:231], v[32:47]\n\t" \
"9:\n\t" \
"ds_read_b64_tr_b16 v[48:49], v72 offset:18432\n\t" \
"ds_read_b64_tr_b16 v[50:51], v72 offset:19200\n\t" \
"ds_read_b64_tr_b16 v[52:53], v72 offset:18464\n\t" \
"ds_read_b64_tr_b16 v[54:55], v72 offset:19232\n\t" \
"ds_read_b64_tr_b16 v[56:57], v72 offset:18496\n\t" \
"ds_read_b64_tr_b16 v[58:59], v72 offset:19264\n\t" \
"s_waitcnt vmcnt(4) lgkmcnt(10)\n\t" \
"v_mfma_f32_32x32x16_bf16 v[0:15], v[60:63], v[232:235], v[0:15]\n\t" \
"s_add_u32 %[so], %[so], 0x1000\n\t" \
"buffer_load_dwordx4 v[196:199], v74, %[rs], %[so] offen offset:0 sc1\n\t" \
"s_waitcnt lgkmcnt(8)\n\t" \
"v_mfma_f32_32x32x16_bf16 v[16:31], v[64:67], v[232:235], v[16:31]\n\t" \
"s_waitcnt lgkmcnt(6)\n\t" \
"s_cbranch_vccz 9f\n\t" \
"v_mfma_f32_32x32x16_bf16 v[32:47], v[68:71], v[232:235], v[32:47]\n\t" \
"9:\n\t" \
"ds_read_b64_tr_b16 v[60:61], v72 offset:19968\n\t" \
"ds_read_b64_tr_b16 v[62:63], v72 offset:20736\n\t" \
"ds_read_b64_tr_b16 v[64:65], v72 offset:20000\n\t" \
"ds_read_b64_tr_b16 v[66:67], v72 offset:20768\n\t" \
"ds_read_b64_tr_b16 v[68:69], v72 offset:20032\n\t" \
"ds_read_b64_tr_b16 v[70:71], v72 offset:20800\n\t" \
"s_waitcnt vmcnt(4) lgkmcnt(10)\n\t" \
"v_mfma_f32_32x32x16_bf16 v[0:15], v[48:51], v[236:239], v[0:15]\n\t" \
"buffer_load_dwordx4 v[200:203], v74, %[rs], %[so] offen offset:1024 sc1\n\t" \
"s_waitcnt lgkmcnt(8)\n\t" \
"v_mfma_f32_32x32x16_bf16 v[16:31], v[52:55], v[236:239], v[16:31]\n\t" \
"s_waitcnt lgkmcnt(6)\n\t" \
"s_cbranch_vccz 9f\n\t" \
"v_mfma_f32_32x32x16_bf16 v[32:47], v[56:59], v[236:239], v[32:47]\n\t" \
"9:\n\t" \
"buffer_load_dwordx4 v[76:79], v75, %[rs], 0 offen sc1\n\t" \
"buffer_load_dwordx4 v[80:83], v75, %[rs], 0 offen offset:16 sc1\n\t" \
"buffer_load_dwordx4 v[244:247], v74, %[rs], %[ro] offen sc1\n\t" \
"ds_read_b64_tr_b16 v[48:49], v72 offset:21504\n\t" \
"ds_read_b64_tr_b16 v[50:51], v72 offset:22272\n\t" \
"ds_read_b64_tr_b16 v[52:53], v72 offset:21536\n\t" \
"ds_read_b64_tr_b16 v[54:55], v72 offset:22304\n\t" \
"ds_read_b64_tr_b16 v[56:57], v72 offset:21568\n\t" \
"ds_read_b64_tr_b16 v[58:59], v72 offset:22336\n\t" \
"s_waitcnt vmcnt(7) lgkmcnt(10)\n\t" \
"v_mfma_f32_32x32x16_bf16 v[0:15], v[60:63], v[240:243], v[0:15]\n\t" \
"buffer_load_dwordx4 v[204:207], v74, %[rs], %[so] offen offset:2048 sc1\n\t" \
"s_waitcnt lgkmcnt(8)\n\t" \
"v_mfma_f32_32x32x16_bf16 v[16:31], v[64:67], v[240:243], v[16:31]\n\t" \
"s_waitcnt lgkmcnt(6)\n\t" \
"s_cbranch_vccz 9f\n\t" \
"v_mfma_f32_32x32x16_bf16 v[32:47], v[68:71], v[240:243], v[32:47]\n\t" \
"9:\n\t" \
"ds_read_b64_tr_b16 v[60:61], v72 offset:23040\n\t" \
"ds_read_b64_tr_b16 v[62:63], v72 offset:23808\n\t" \
"ds_read_b64_tr_b16 v[64:65], v72 offset:23072\n\t" \
"ds_read_b64_tr_b16 v[66:67], v72 offset:23840\n\t" \
"ds_read_b64_tr_b16 v[68:69], v72 offset:23104\n\t" \
"ds_read_b64_tr_b16 v[70:71], v72 offset:23872\n\t" \
"s_waitcnt vmcnt(7) lgkmcnt(10)\n\t" \
"v_mfma_f32_32x32x16_bf16 v[0:15], v[48:51], v[188:191], v[0:15]\n\t" \
"buffer_load_dwordx4 v[208:211], v74, %[rs], %[so] offen offset:3072 sc1\n\t" \
"s_waitcnt lgkmcnt(8)\n\t" \
"v_mfma_f32_32x32x16_bf16 v[16:31], v[52:55], v[188:191], v[16:31]\n\t" \
"s_waitcnt lgkmcnt(6)\n\t" \
"s_cbranch_vccz 9f\n\t" \
"v_mfma_f32_32x32x16_bf16 v[32:47], v[56:59], v[188:191], v[32:47]\n\t" \
"9:\n\t" \
"s_mov_b32 %[keep], m0\n\t" \
"s_mov_b32 m0, %[el]\n\t" \
"s_nop 0\n\t" \
"buffer_load_dwordx4 v73, %[wr], %[eo] offen lds\n\t" \
"ds_read_b64_tr_b16 v[48:49], v72 offset:24576\n\t" \
"ds_read_b64_tr_b16 v[50:51], v72 offset:25344\n\t" \
"ds_read_b64_tr_b16 v[52:53], v72 offset:24608\n\t" \
"ds_read_b64_tr_b16 v[54:55], v72 offset:25376\n\t" \
"ds_read_b64_tr_b16 v[56:57], v72 offset:24640\n\t" \
"ds_read_b64_tr_b16 v[58:59], v72 offset:25408\n\t" \
"s_waitcnt vmcnt(8) lgkmcnt(10)\n\t" \
"v_mfma_f32_32x32x16_bf16 v[0:15], v[60:63], v[192:195], v[0:15]\n\t" \
"s_add_u32 %[so], %[so], 0x1000\n\t" \
"buffer_load_dwordx4 v[212:215], v74, %[rs], %[so] offen offset:0 sc1\n\t" \
"s_waitcnt lgkmcnt(8)\n\t" \
"v_mfma_f32_32x32x16_bf16 v[16:31], v[64:67], v[192:195], v[16:31]\n\t" \
"s_waitcnt lgkmcnt(6)\n\t" \
"s_cbranch_vccz 9f\n\t" \
"v_mfma_f32_32x32x16_bf16 v[32:47], v[68:71], v[192:195], v[32:47]\n\t" \
"9:\n\t" \
"ds_read_b64_tr_b16 v[60:61], v72 offset:26112\n\t" \
"ds_read_b64_tr_b16 v[62:63], v72 offset:26880\n\t" \
"ds_read_b64_tr_b16 v[64:65], v72 offset:26144\n\t" \
"ds_read_b64_tr_b16 v[66:67], v72 offset:26912\n\t" \
"ds_read_b64_tr_b16 v[68:69], v72 offset:26176\n\t" \
"ds_read_b64_tr_b16 v[70:71], v72 offset:26944\n\t" \
"s_waitcnt vmcnt(8) lgkmcnt(10)\n\t" \
"v_mfma_f32_32x32x16_bf16 v[0:15], v[48:51], v[196:199], v[0:15]\n\t" \
"buffer_load_dwordx4 v[216:219], v74, %[rs], %[so] offen offset:1024 sc1\n\t" \
"s_waitcnt lgkmcnt(8)\n\t" \
"v_mfma_f32_32x32x16_bf16 v[16:31], v[52:55], v[196:199], v[16:31]\n\t" \
"s_waitcnt lgkmcnt(6)\n\t" \
"s_cbranch_vccz 9f\n\t" \
"v_mfma_f32_32x32x16_bf16 v[32:47], v[56:59], v[196:199], v[32:47]\n\t" \
"9:\n\t" \
"s_add_u32 %[eo], %[eo], 0x4000\n\t" \
"s_add_u32 m0, m0, 0x2000\n\t" \
"s_nop 0\n\t" \
"buffer_load_dwordx4 v73, %[wr], %[eo] offen lds\n\t" \
"ds_read_b64_tr_b16 v[48:49], v72 offset:27648\n\t" \
"ds_read_b64_tr_b16 v[50:51], v72 offset:28416\n\t" \
"ds_read_b64_tr_b16 v[52:53], v72 offset:27680\n\t" \
"ds_read_b64_tr_b16 v[54:55], v72 offset:28448\n\t" \
"ds_read_b64_tr_b16 v[56:57], v72 offset:27712\n\t" \
"ds_read_b64_tr_b16 v[58:59], v72 offset:28480\n\t" \
"s_waitcnt vmcnt(9) lgkmcnt(10)\n\t" \
"v_mfma_f32_32x32x16_bf16 v[0:15], v[60:63], v[200:203], v[0:15]\n\t" \
"buffer_load_dwordx4 v[220:223], v74, %[rs], %[so] offen offset:2048 sc1\n\t" \
"s_waitcnt lgkmcnt(8)\n\t" \
"v_mfma_f32_32x32x16_bf16 v[16:31], v[64:67], v[200:203], v[16:31]\n\t" \
"s_waitcnt lgkmcnt(6)\n\t" \
"s_cbranch_vccz 9f\n\t" \
"v_mfma_f32_32x32x16_bf16 v[32:47], v[68:71], v[200:203], v[32:47]\n\t" \
"9:\n\t" \
"ds_read_b64_tr_b16 v[60:61], v72 offset:29184\n\t" \
"ds_read_b64_tr_b16 v[62:63], v72 offset:29952\n\t" \
"ds_read_b64_tr_b16 v[64:65], v72 offset:29216\n\t" \
"ds_read_b64_tr_b16 v[66:67], v72 offset:29984\n\t" \
"ds_read_b64_tr_b16 v[68:69], v72 offset:29248\n\t" \
"ds_read_b64_tr_b16 v[70:71], v72 offset:30016\n\t" \
"s_waitcnt vmcnt(6) lgkmcnt(10)\n\t" \
"v_mfma_f32_32x32x16_bf16 v[0:15], v[48:51], v[204:207], v[0:15]\n\t" \
"buffer_load_dwordx4 v[224:227], v74, %[rs], %[so] offen offset:3072 sc1\n\t" \
"s_waitcnt lgkmcnt(8)\n\t" \
"v_mfma_f32_32x32x16_bf16 v[16:31], v[52:55], v[204:207], v[16:31]\n\t" \
"s_add_u32 %[so], %[so], 0x1000\n\t" \
"buffer_load_dwordx4 v[228:231], v74, %[rs], %[so] offen offset:0 sc1\n\t" \
"s_waitcnt lgkmcnt(6)\n\t" \
"s_cbranch_vccz 9f\n\t" \
"v_mfma_f32_32x32x16_bf16 v[32:47], v[56:59], v[204:207], v[32:47]\n\t" \
"9:\n\t" \
"s_add_u32 %[eo], %[eo], 0x4000\n\t" \
"s_add_u32 m0, m0, 0x2000\n\t" \
"s_nop 0\n\t" \
"buffer_load_dwordx4 v73, %[wr], %[eo] offen lds\n\t" \
"ds_read_b64_tr_b16 v[48:49], v72 offset:30720\n\t" \
"ds_read_b64_tr_b16 v[50:51], v72 offset:31488\n\t" \
"ds_read_b64_tr_b16 v[52:53], v72 offset:30752\n\t" \
"ds_read_b64_tr_b16 v[54:55], v72 offset:31520\n\t" \
"ds_read_b64_tr_b16 v[56:57], v72 offset:30784\n\t" \
"ds_read_b64_tr_b16 v[58:59], v72 offset:31552\n\t" \
"s_waitcnt vmcnt(8) lgkmcnt(10)\n\t" \
"v_mfma_f32_32x32x16_bf16 v[0:15], v[60:63], v[208:211], v[0:15]\n\t" \
"buffer_load_dwordx4 v[232:235], v74, %[rs], %[so] offen offset:1024 sc1\n\t" \
"s_waitcnt lgkmcnt(8)\n\t" \
"v_mfma_f32_32x32x16_bf16 v[16:31], v[64:67], v[208:211], v[16:31]\n\t" \
"buffer_load_dwordx4 v[236:239], v74, %[rs], %[so] offen offset:2048 sc1\n\t" \
"s_waitcnt lgkmcnt(6)\n\t" \
"s_cbranch_vccz 9f\n\t" \
"v_mfma_f32_32x32x16_bf16 v[32:47], v[68:71], v[208:211], v[32:47]\n\t" \
"9:\n\t" \
"ds_read_b64_tr_b16 v[60:61], v72 offset:32256\n\t" \
"ds_read_b64_tr_b16 v[62:63], v72 offset:33024\n\t" \
"ds_read_b64_tr_b16 v[64:65], v72 offset:32288\n\t" \
"ds_read_b64_tr_b16 v[66:67], v72 offset:33056\n\t" \
"ds_read_b64_tr_b16 v[68:69], v72 offset:32320\n\t" \
"ds_read_b64_tr_b16 v[70:71], v72 offset:33088\n\t" \
"s_waitcnt vmcnt(8) lgkmcnt(10)\n\t" \
"v_mfma_f32_32x32x16_bf16 v[0:15], v[48:51], v[212:215], v[0:15]\n\t" \
"buffer_load_dwordx4 v[240:243], v74, %[rs], %[so] offen offset:3072 sc1\n\t" \
"s_waitcnt lgkmcnt(8)\n\t" \
"v_mfma_f32_32x32x16_bf16 v[16:31], v[52:55], v[212:215], v[16:31]\n\t" \
"s_add_u32 %[so], %[so], 0x1000\n\t" \
"buffer_load_dwordx4 v[188:191], v74, %[rs], %[so] offen offset:0 sc1\n\t" \
"s_waitcnt lgkmcnt(6)\n\t" \
"s_cbranch_vccz 9f\n\t" \
"v_mfma_f32_32x32x16_bf16 v[32:47], v[56:59], v[212:215], v[32:47]\n\t" \
"9:\n\t" \
"s_add_u32 %[eo], %[eo], 0x4000\n\t" \
"s_add_u32 m0, m0, 0x2000\n\t" \
"s_nop 0\n\t" \
"buffer_load_dwordx4 v73, %[wr], %[eo] offen lds\n\t" \
"ds_read_b64_tr_b16 v[48:49], v72 offset:33792\n\t" \
"ds_read_b64_tr_b16 v[50:51], v72 offset:34560\n\t" \
"ds_read_b64_tr_b16 v[52:53], v72 offset:33824\n\t" \
"ds_read_b64_tr_b16 v[54:55], v72 offset:34592\n\t" \
"ds_read_b64_tr_b16 v[56:57], v72 offset:33856\n\t" \
"ds_read_b64_tr_b16 v[58:59], v72 offset:34624\n\t" \
"s_waitcnt vmcnt(10) lgkmcnt(10)\n\t" \
"v_mfma_f32_32x32x16_bf16 v[0:15], v[60:63], v[216:219], v[0:15]\n\t" \
"buffer_load_dwordx4 v[192:195], v74, %[rs], %[so] offen offset:1024 sc1\n\t" \
"v_add_f32 v76, v76, v78\n\t" \
"v_add_f32 v80, v80, v82\n\t" \
"v_add_f32 v77, v77, v79\n\t" \
"v_add_f32 v81, v81, v83\n\t" \
"v_add_f32 v76, v76, v80\n\t" \
"v_add_f32 v77, v77, v81\n\t" \
"v_mul_f32 v76, 0x3b000000, v76\n\t" \
"v_mul_f32 v77, 0x3b000000, v77\n\t" \
"v_fma_f32 v77, -v76, v76, v77\n\t" \
"v_max_f32 v77, 0, v77\n\t" \
"v_add_f32 v77, 0x3727c5ac, v77\n\t" \
"v_rsq_f32 v122, v77\n\t" \
"s_nop 0\n\t" \
"v_mul_f32 v120, 0xbfb8aa3b, v122\n\t" \
"v_mul_f32 v123, -v76, v122\n\t" \
"v_mul_f32 v121, -v76, v120\n\t" \
"s_waitcnt lgkmcnt(8)\n\t" \
"v_mfma_f32_32x32x16_bf16 v[16:31], v[64:67], v[216:219], v[16:31]\n\t" \
"buffer_load_dwordx4 v[196:199], v74, %[rs], %[so] offen offset:2048 sc1\n\t" \
"s_waitcnt lgkmcnt(6)\n\t" \
"s_cbranch_vccz 9f\n\t" \
"v_mfma_f32_32x32x16_bf16 v[32:47], v[68:71], v[216:219], v[32:47]\n\t" \
"9:\n\t" \
"ds_read_b64_tr_b16 v[60:61], v72 offset:35328\n\t" \
"ds_read_b64_tr_b16 v[62:63], v72 offset:36096\n\t" \
"ds_read_b64_tr_b16 v[64:65], v72 offset:35360\n\t" \
"ds_read_b64_tr_b16 v[66:67], v72 offset:36128\n\t" \
"ds_read_b64_tr_b16 v[68:69], v72 offset:35392\n\t" \
"ds_read_b64_tr_b16 v[70:71], v72 offset:36160\n\t" \
"s_waitcnt vmcnt(10) lgkmcnt(10)\n\t" \
"v_mfma_f32_32x32x16_bf16 v[0:15], v[48:51], v[220:223], v[0:15]\n\t" \
"buffer_load_dwordx4 v[200:203], v74, %[rs], %[so] offen offset:3072 sc1\n\t" \
"s_waitcnt lgkmcnt(8)\n\t" \
"v_mfma_f32_32x32x16_bf16 v[16:31], v[52:55], v[220:223], v[16:31]\n\t" \
"s_waitcnt lgkmcnt(6)\n\t" \
"s_cbranch_vccz 9f\n\t" \
"v_mfma_f32_32x32x16_bf16 v[32:47], v[56:59], v[220:223], v[32:47]\n\t" \
"9:\n\t" \
"s_add_u32 %[eo], %[eo], 0x4000\n\t" \
"s_add_u32 m0, m0, 0x2000\n\t" \
"s_nop 0\n\t" \
"buffer_load_dwordx4 v73, %[wr], %[eo] offen lds\n\t" \
"s_mov_b32 m0, %[keep]\n\t" \
"ds_read_b64_tr_b16 v[48:49], v72 offset:36864\n\t" \
"ds_read_b64_tr_b16 v[50:51], v72 offset:37632\n\t" \
"ds_read_b64_tr_b16 v[52:53], v72 offset:38400\n\t" \
"ds_read_b64_tr_b16 v[54:55], v72 offset:39168\n\t" \
"ds_read_b64_tr_b16 v[56:57], v72 offset:39936\n\t" \
"ds_read_b64_tr_b16 v[58:59], v72 offset:40704\n\t" \
"s_waitcnt vmcnt(11) lgkmcnt(10)\n\t" \
"v_mfma_f32_32x32x16_bf16 v[0:15], v[60:63], v[224:227], v[0:15]\n\t" \
"ds_read_b64_tr_b16 v[60:61], v72 offset:41472\n\t" \
"ds_read_b64_tr_b16 v[62:63], v72 offset:42240\n\t" \
"s_waitcnt lgkmcnt(10)\n\t" \
"v_mfma_f32_32x32x16_bf16 v[16:31], v[64:67], v[224:227], v[16:31]\n\t" \
"ds_read_b64_tr_b16 v[64:65], v72 offset:43008\n\t" \
"ds_read_b64_tr_b16 v[66:67], v72 offset:43776\n\t" \
"s_waitcnt lgkmcnt(10)\n\t" \
"s_cbranch_vccz 9f\n\t" \
"v_mfma_f32_32x32x16_bf16 v[32:47], v[68:71], v[224:227], v[32:47]\n\t" \
"9:\n\t" \
"s_waitcnt vmcnt(10) lgkmcnt(8)\n\t" \
"v_mfma_f32_32x32x16_bf16 v[0:15], v[48:51], v[228:231], v[0:15]\n\t" \
"ds_read_b64_tr_b16 v[68:69], v72 offset:44544\n\t" \
"ds_read_b64_tr_b16 v[70:71], v72 offset:45312\n\t" \
"s_waitcnt vmcnt(8) lgkmcnt(8)\n\t" \
"v_mfma_f32_32x32x16_bf16 v[0:15], v[52:55], v[232:235], v[0:15]\n\t" \
"ds_read_b64_tr_b16 v[48:49], v72 offset:46080\n\t" \
"ds_read_b64_tr_b16 v[50:51], v72 offset:46848\n\t" \
"s_waitcnt vmcnt(7) lgkmcnt(8)\n\t" \
"v_mfma_f32_32x32x16_bf16 v[0:15], v[56:59], v[236:239], v[0:15]\n\t" \
"ds_read_b64_tr_b16 v[52:53], v72 offset:47616\n\t" \
"ds_read_b64_tr_b16 v[54:55], v72 offset:48384\n\t" \
"s_waitcnt vmcnt(6) lgkmcnt(8)\n\t" \
"v_mfma_f32_32x32x16_bf16 v[0:15], v[60:63], v[240:243], v[0:15]\n\t" \
"ds_read_b64_tr_b16 v[56:57], v72 offset:36896\n\t" \
"ds_read_b64_tr_b16 v[58:59], v72 offset:37664\n\t" \
"s_waitcnt vmcnt(5) lgkmcnt(8)\n\t" \
"v_mfma_f32_32x32x16_bf16 v[0:15], v[64:67], v[188:191], v[0:15]\n\t" \
"ds_read_b64_tr_b16 v[60:61], v72 offset:38432\n\t" \
"ds_read_b64_tr_b16 v[62:63], v72 offset:39200\n\t" \
"s_waitcnt vmcnt(3) lgkmcnt(8)\n\t" \
"v_mfma_f32_32x32x16_bf16 v[0:15], v[68:71], v[192:195], v[0:15]\n\t" \
"ds_read_b64_tr_b16 v[64:65], v72 offset:39968\n\t" \
"ds_read_b64_tr_b16 v[66:67], v72 offset:40736\n\t" \
"s_waitcnt vmcnt(2) lgkmcnt(8)\n\t" \
"v_mfma_f32_32x32x16_bf16 v[0:15], v[48:51], v[196:199], v[0:15]\n\t" \
"ds_read_b64_tr_b16 v[68:69], v72 offset:41504\n\t" \
"ds_read_b64_tr_b16 v[70:71], v72 offset:42272\n\t" \
"s_waitcnt vmcnt(1) lgkmcnt(8)\n\t" \
"v_mfma_f32_32x32x16_bf16 v[0:15], v[52:55], v[200:203], v[0:15]\n\t" \
"ds_read_b64_tr_b16 v[48:49], v72 offset:43040\n\t" \
"ds_read_b64_tr_b16 v[50:51], v72 offset:43808\n\t" \
"ds_read_b128 v[84:87], v124 offset:0\n\t" \
"ds_read_b128 v[88:91], v124 offset:16\n\t" \
"ds_read_b128 v[92:95], v124 offset:32\n\t" \
"ds_read_b128 v[96:99], v124 offset:48\n\t" \
"s_waitcnt lgkmcnt(12)\n\t" \
"v_mfma_f32_32x32x16_bf16 v[16:31], v[56:59], v[228:231], v[16:31]\n\t" \
"ds_read_b64_tr_b16 v[52:53], v72 offset:44576\n\t" \
"ds_read_b64_tr_b16 v[54:55], v72 offset:45344\n\t" \
"s_waitcnt lgkmcnt(8)\n\t" \
"v_mfma_f32_32x32x16_bf16 v[16:31], v[60:63], v[232:235], v[16:31]\n\t" \
"ds_read_b64_tr_b16 v[56:57], v72 offset:46112\n\t" \
"ds_read_b64_tr_b16 v[58:59], v72 offset:46880\n\t" \
"s_waitcnt lgkmcnt(4)\n\t" \
"v_mul_f32 v100, v84, v121\n\t" \
"v_mul_f32 v102, v85, v121\n\t" \
"v_mul_f32 v101, v92, v123\n\t" \
"v_mul_f32 v103, v93, v123\n\t" \
"v_fmac_f32 v100, v0, v120\n\t" \
"v_fmac_f32 v102, v1, v120\n\t" \
"v_fmac_f32 v101, v8, v122\n\t" \
"v_fmac_f32 v103, v9, v122\n\t" \
"v_exp_f32 v100, v100\n\t" \
"v_exp_f32 v102, v102\n\t" \
"s_waitcnt lgkmcnt(8)\n\t" \
"v_mfma_f32_32x32x16_bf16 v[16:31], v[64:67], v[236:239], v[16:31]\n\t" \
"ds_read_b64_tr_b16 v[60:61], v72 offset:47648\n\t" \
"ds_read_b64_tr_b16 v[62:63], v72 offset:48416\n\t" \
"v_add_f32 v100, 1.0, v100\n\t" \
"v_add_f32 v102, 1.0, v102\n\t" \
"v_rcp_f32 v100, v100\n\t" \
"v_rcp_f32 v102, v102\n\t" \
"v_mul_f32 v108, v100, v101\n\t" \
"v_mul_f32 v109, v102, v103\n\t" \
"v_mul_f32 v104, v86, v121\n\t" \
"v_mul_f32 v106, v87, v121\n\t" \
"v_mul_f32 v105, v94, v123\n\t" \
"v_mul_f32 v107, v95, v123\n\t" \
"s_waitcnt lgkmcnt(8)\n\t" \
"v_mfma_f32_32x32x16_bf16 v[16:31], v[68:71], v[240:243], v[16:31]\n\t" \
"ds_read_b64_tr_b16 v[64:65], v72 offset:36928\n\t" \
"ds_read_b64_tr_b16 v[66:67], v72 offset:37696\n\t" \
"v_fmac_f32 v104, v2, v120\n\t" \
"v_fmac_f32 v106, v3, v120\n\t" \
"v_fmac_f32 v105, v10, v122\n\t" \
"v_fmac_f32 v107, v11, v122\n\t" \
"v_exp_f32 v104, v104\n\t" \
"v_exp_f32 v106, v106\n\t" \
"v_add_f32 v104, 1.0, v104\n\t" \
"v_add_f32 v106, 1.0, v106\n\t" \
"v_rcp_f32 v104, v104\n\t" \
"v_rcp_f32 v106, v106\n\t" \
"s_waitcnt lgkmcnt(8)\n\t" \
"v_mfma_f32_32x32x16_bf16 v[16:31], v[48:51], v[188:191], v[16:31]\n\t" \
"ds_read_b64_tr_b16 v[68:69], v72 offset:38464\n\t" \
"ds_read_b64_tr_b16 v[70:71], v72 offset:39232\n\t" \
"v_mul_f32 v110, v104, v105\n\t" \
"v_mul_f32 v111, v106, v107\n\t" \
"v_mul_f32 v100, v88, v121\n\t" \
"v_mul_f32 v102, v89, v121\n\t" \
"v_mul_f32 v101, v96, v123\n\t" \
"v_mul_f32 v103, v97, v123\n\t" \
"v_fmac_f32 v100, v4, v120\n\t" \
"v_fmac_f32 v102, v5, v120\n\t" \
"v_fmac_f32 v101, v12, v122\n\t" \
"v_fmac_f32 v103, v13, v122\n\t" \
"s_waitcnt lgkmcnt(8)\n\t" \
"v_mfma_f32_32x32x16_bf16 v[16:31], v[52:55], v[192:195], v[16:31]\n\t" \
"ds_read_b64_tr_b16 v[48:49], v72 offset:40000\n\t" \
"ds_read_b64_tr_b16 v[50:51], v72 offset:40768\n\t" \
"v_exp_f32 v100, v100\n\t" \
"v_exp_f32 v102, v102\n\t" \
"v_add_f32 v100, 1.0, v100\n\t" \
"v_add_f32 v102, 1.0, v102\n\t" \
"v_rcp_f32 v100, v100\n\t" \
"v_rcp_f32 v102, v102\n\t" \
"v_mul_f32 v112, v100, v101\n\t" \
"v_mul_f32 v113, v102, v103\n\t" \
"v_mul_f32 v104, v90, v121\n\t" \
"v_mul_f32 v106, v91, v121\n\t" \
"s_waitcnt lgkmcnt(8)\n\t" \
"v_mfma_f32_32x32x16_bf16 v[16:31], v[56:59], v[196:199], v[16:31]\n\t" \
"ds_read_b64_tr_b16 v[52:53], v72 offset:41536\n\t" \
"ds_read_b64_tr_b16 v[54:55], v72 offset:42304\n\t" \
"v_mul_f32 v105, v98, v123\n\t" \
"v_mul_f32 v107, v99, v123\n\t" \
"v_fmac_f32 v104, v6, v120\n\t" \
"v_fmac_f32 v106, v7, v120\n\t" \
"v_fmac_f32 v105, v14, v122\n\t" \
"v_fmac_f32 v107, v15, v122\n\t" \
"v_exp_f32 v104, v104\n\t" \
"v_exp_f32 v106, v106\n\t" \
"v_add_f32 v104, 1.0, v104\n\t" \
"v_add_f32 v106, 1.0, v106\n\t" \
"s_waitcnt lgkmcnt(8)\n\t" \
"v_mfma_f32_32x32x16_bf16 v[16:31], v[60:63], v[200:203], v[16:31]\n\t" \
"ds_read_b64_tr_b16 v[56:57], v72 offset:43072\n\t" \
"ds_read_b64_tr_b16 v[58:59], v72 offset:43840\n\t" \
"ds_read_b128 v[84:87], v124 offset:128\n\t" \
"ds_read_b128 v[88:91], v124 offset:144\n\t" \
"ds_read_b128 v[92:95], v124 offset:160\n\t" \
"ds_read_b128 v[96:99], v124 offset:176\n\t" \
"v_rcp_f32 v104, v104\n\t" \
"v_rcp_f32 v106, v106\n\t" \
"v_mul_f32 v114, v104, v105\n\t" \
"v_mul_f32 v115, v106, v107\n\t" \
"v_cvt_pk_bf16_f32 v116, v108, v109\n\t" \
"v_cvt_pk_bf16_f32 v117, v110, v111\n\t" \
"v_cvt_pk_bf16_f32 v118, v112, v113\n\t" \
"v_cvt_pk_bf16_f32 v119, v114, v115\n\t" \
"buffer_store_dwordx4 v[116:119], v74, %[rs], %[ao] offen offset:0 sc1\n\t" \
"s_cbranch_vccz 8f\n\t" \
"s_waitcnt lgkmcnt(12)\n\t" \
"v_mfma_f32_32x32x16_bf16 v[32:47], v[64:67], v[228:231], v[32:47]\n\t" \
"ds_read_b64_tr_b16 v[60:61], v72 offset:44608\n\t" \
"ds_read_b64_tr_b16 v[62:63], v72 offset:45376\n\t" \
"s_waitcnt lgkmcnt(8)\n\t" \
"v_mfma_f32_32x32x16_bf16 v[32:47], v[68:71], v[232:235], v[32:47]\n\t" \
"ds_read_b64_tr_b16 v[64:65], v72 offset:46144\n\t" \
"ds_read_b64_tr_b16 v[66:67], v72 offset:46912\n\t" \
"s_waitcnt lgkmcnt(4)\n\t" \
"v_mul_f32 v100, v84, v121\n\t" \
"v_mul_f32 v102, v85, v121\n\t" \
"v_mul_f32 v101, v92, v123\n\t" \
"v_mul_f32 v103, v93, v123\n\t" \
"v_fmac_f32 v100, v16, v120\n\t" \
"v_fmac_f32 v102, v17, v120\n\t" \
"v_fmac_f32 v101, v24, v122\n\t" \
"v_fmac_f32 v103, v25, v122\n\t" \
"v_exp_f32 v100, v100\n\t" \
"v_exp_f32 v102, v102\n\t" \
"s_waitcnt lgkmcnt(8)\n\t" \
"v_mfma_f32_32x32x16_bf16 v[32:47], v[48:51], v[236:239], v[32:47]\n\t" \
"ds_read_b64_tr_b16 v[68:69], v72 offset:47680\n\t" \
"ds_read_b64_tr_b16 v[70:71], v72 offset:48448\n\t" \
"v_add_f32 v100, 1.0, v100\n\t" \
"v_add_f32 v102, 1.0, v102\n\t" \
"v_rcp_f32 v100, v100\n\t" \
"v_rcp_f32 v102, v102\n\t" \
"v_mul_f32 v108, v100, v101\n\t" \
"v_mul_f32 v109, v102, v103\n\t" \
"v_mul_f32 v104, v86, v121\n\t" \
"v_mul_f32 v106, v87, v121\n\t" \
"v_mul_f32 v105, v94, v123\n\t" \
"v_mul_f32 v107, v95, v123\n\t" \
"s_waitcnt lgkmcnt(8)\n\t" \
"v_mfma_f32_32x32x16_bf16 v[32:47], v[52:55], v[240:243], v[32:47]\n\t" \
"v_fmac_f32 v104, v18, v120\n\t" \
"v_fmac_f32 v106, v19, v120\n\t" \
"v_fmac_f32 v105, v26, v122\n\t" \
"v_fmac_f32 v107, v27, v122\n\t" \
"v_exp_f32 v104, v104\n\t" \
"v_exp_f32 v106, v106\n\t" \
"v_add_f32 v104, 1.0, v104\n\t" \
"v_add_f32 v106, 1.0, v106\n\t" \
"v_rcp_f32 v104, v104\n\t" \
"v_rcp_f32 v106, v106\n\t" \
"s_waitcnt lgkmcnt(6)\n\t" \
"v_mfma_f32_32x32x16_bf16 v[32:47], v[56:59], v[188:191], v[32:47]\n\t" \
"v_mul_f32 v110, v104, v105\n\t" \
"v_mul_f32 v111, v106, v107\n\t" \
"v_mul_f32 v100, v88, v121\n\t" \
"v_mul_f32 v102, v89, v121\n\t" \
"v_mul_f32 v101, v96, v123\n\t" \
"v_mul_f32 v103, v97, v123\n\t" \
"v_fmac_f32 v100, v20, v120\n\t" \
"v_fmac_f32 v102, v21, v120\n\t" \
"v_fmac_f32 v101, v28, v122\n\t" \
"v_fmac_f32 v103, v29, v122\n\t" \
"s_waitcnt lgkmcnt(4)\n\t" \
"v_mfma_f32_32x32x16_bf16 v[32:47], v[60:63], v[192:195], v[32:47]\n\t" \
"v_exp_f32 v100, v100\n\t" \
"v_exp_f32 v102, v102\n\t" \
"v_add_f32 v100, 1.0, v100\n\t" \
"v_add_f32 v102, 1.0, v102\n\t" \
"v_rcp_f32 v100, v100\n\t" \
"v_rcp_f32 v102, v102\n\t" \
"v_mul_f32 v112, v100, v101\n\t" \
"v_mul_f32 v113, v102, v103\n\t" \
"v_mul_f32 v104, v90, v121\n\t" \
"v_mul_f32 v106, v91, v121\n\t" \
"s_waitcnt lgkmcnt(2)\n\t" \
"v_mfma_f32_32x32x16_bf16 v[32:47], v[64:67], v[196:199], v[32:47]\n\t" \
"v_mul_f32 v105, v98, v123\n\t" \
"v_mul_f32 v107, v99, v123\n\t" \
"v_fmac_f32 v104, v22, v120\n\t" \
"v_fmac_f32 v106, v23, v120\n\t" \
"v_fmac_f32 v105, v30, v122\n\t" \
"v_fmac_f32 v107, v31, v122\n\t" \
"v_exp_f32 v104, v104\n\t" \
"v_exp_f32 v106, v106\n\t" \
"v_add_f32 v104, 1.0, v104\n\t" \
"v_add_f32 v106, 1.0, v106\n\t" \
"s_waitcnt lgkmcnt(0)\n\t" \
"v_mfma_f32_32x32x16_bf16 v[32:47], v[68:71], v[200:203], v[32:47]\n\t" \
"v_rcp_f32 v104, v104\n\t" \
"v_rcp_f32 v106, v106\n\t" \
"v_mul_f32 v114, v104, v105\n\t" \
"v_mul_f32 v115, v106, v107\n\t" \
"v_cvt_pk_bf16_f32 v116, v108, v109\n\t" \
"v_cvt_pk_bf16_f32 v117, v110, v111\n\t" \
"v_cvt_pk_bf16_f32 v118, v112, v113\n\t" \
"v_cvt_pk_bf16_f32 v119, v114, v115\n\t" \
"buffer_store_dwordx4 v[116:119], v74, %[rs], %[ao] offen offset:1024 sc1\n\t" \
"ds_read_b128 v[84:87], v124 offset:256\n\t" \
"ds_read_b128 v[88:91], v124 offset:272\n\t" \
"ds_read_b128 v[92:95], v124 offset:288\n\t" \
"ds_read_b128 v[96:99], v124 offset:304\n\t" \
"s_nop 7\n\t" \
"s_waitcnt lgkmcnt(0)\n\t" \
"v_mul_f32 v100, v84, v121\n\t" \
"v_mul_f32 v102, v85, v121\n\t" \
"v_mul_f32 v101, v92, v123\n\t" \
"v_mul_f32 v103, v93, v123\n\t" \
"v_fmac_f32 v100, v32, v120\n\t" \
"v_fmac_f32 v102, v33, v120\n\t" \
"v_fmac_f32 v101, v40, v122\n\t" \
"v_fmac_f32 v103, v41, v122\n\t" \
"v_exp_f32 v100, v100\n\t" \
"v_exp_f32 v102, v102\n\t" \
"v_add_f32 v100, 1.0, v100\n\t" \
"v_add_f32 v102, 1.0, v102\n\t" \
"v_rcp_f32 v100, v100\n\t" \
"v_rcp_f32 v102, v102\n\t" \
"v_mul_f32 v108, v100, v101\n\t" \
"v_mul_f32 v109, v102, v103\n\t" \
"v_mul_f32 v104, v86, v121\n\t" \
"v_mul_f32 v106, v87, v121\n\t" \
"v_mul_f32 v105, v94, v123\n\t" \
"v_mul_f32 v107, v95, v123\n\t" \
"v_fmac_f32 v104, v34, v120\n\t" \
"v_fmac_f32 v106, v35, v120\n\t" \
"v_fmac_f32 v105, v42, v122\n\t" \
"v_fmac_f32 v107, v43, v122\n\t" \
"v_exp_f32 v104, v104\n\t" \
"v_exp_f32 v106, v106\n\t" \
"v_add_f32 v104, 1.0, v104\n\t" \
"v_add_f32 v106, 1.0, v106\n\t" \
"v_rcp_f32 v104, v104\n\t" \
"v_rcp_f32 v106, v106\n\t" \
"v_mul_f32 v110, v104, v105\n\t" \
"v_mul_f32 v111, v106, v107\n\t" \
"v_mul_f32 v100, v88, v121\n\t" \
"v_mul_f32 v102, v89, v121\n\t" \
"v_mul_f32 v101, v96, v123\n\t" \
"v_mul_f32 v103, v97, v123\n\t" \
"v_fmac_f32 v100, v36, v120\n\t" \
"v_fmac_f32 v102, v37, v120\n\t" \
"v_fmac_f32 v101, v44, v122\n\t" \
"v_fmac_f32 v103, v45, v122\n\t" \
"v_exp_f32 v100, v100\n\t" \
"v_exp_f32 v102, v102\n\t" \
"v_add_f32 v100, 1.0, v100\n\t" \
"v_add_f32 v102, 1.0, v102\n\t" \
"v_rcp_f32 v100, v100\n\t" \
"v_rcp_f32 v102, v102\n\t" \
"v_mul_f32 v112, v100, v101\n\t" \
"v_mul_f32 v113, v102, v103\n\t" \
"v_mul_f32 v104, v90, v121\n\t" \
"v_mul_f32 v106, v91, v121\n\t" \
"v_mul_f32 v105, v98, v123\n\t" \
"v_mul_f32 v107, v99, v123\n\t" \
"v_fmac_f32 v104, v38, v120\n\t" \
"v_fmac_f32 v106, v39, v120\n\t" \
"v_fmac_f32 v105, v46, v122\n\t" \
"v_fmac_f32 v107, v47, v122\n\t" \
"v_exp_f32 v104, v104\n\t" \
"v_exp_f32 v106, v106\n\t" \
"v_add_f32 v104, 1.0, v104\n\t" \
"v_add_f32 v106, 1.0, v106\n\t" \
"v_rcp_f32 v104, v104\n\t" \
"v_rcp_f32 v106, v106\n\t" \
"v_mul_f32 v114, v104, v105\n\t" \
"v_mul_f32 v115, v106, v107\n\t" \
"v_cvt_pk_bf16_f32 v116, v108, v109\n\t" \
"v_cvt_pk_bf16_f32 v117, v110, v111\n\t" \
"v_cvt_pk_bf16_f32 v118, v112, v113\n\t" \
"v_cvt_pk_bf16_f32 v119, v114, v115\n\t" \
"buffer_store_dwordx4 v[116:119], v74, %[rs], %[ao] offen offset:2048 sc1\n\t" \
"s_branch 7f\n\t" \
"8:\n\t" \
"s_waitcnt lgkmcnt(0)\n\t" \
"s_nop 7\n\t" \
"v_mul_f32 v100, v84, v121\n\t" \
"v_mul_f32 v102, v85, v121\n\t" \
"v_mul_f32 v101, v92, v123\n\t" \
"v_mul_f32 v103, v93, v123\n\t" \
"v_fmac_f32 v100, v16, v120\n\t" \
"v_fmac_f32 v102, v17, v120\n\t" \
"v_fmac_f32 v101, v24, v122\n\t" \
"v_fmac_f32 v103, v25, v122\n\t" \
"v_exp_f32 v100, v100\n\t" \
"v_exp_f32 v102, v102\n\t" \
"v_add_f32 v100, 1.0, v100\n\t" \
"v_add_f32 v102, 1.0, v102\n\t" \
"v_rcp_f32 v100, v100\n\t" \
"v_rcp_f32 v102, v102\n\t" \
"v_mul_f32 v108, v100, v101\n\t" \
"v_mul_f32 v109, v102, v103\n\t" \
"v_mul_f32 v104, v86, v121\n\t" \
"v_mul_f32 v106, v87, v121\n\t" \
"v_mul_f32 v105, v94, v123\n\t" \
"v_mul_f32 v107, v95, v123\n\t" \
"v_fmac_f32 v104, v18, v120\n\t" \
"v_fmac_f32 v106, v19, v120\n\t" \
"v_fmac_f32 v105, v26, v122\n\t" \
"v_fmac_f32 v107, v27, v122\n\t" \
"v_exp_f32 v104, v104\n\t" \
"v_exp_f32 v106, v106\n\t" \
"v_add_f32 v104, 1.0, v104\n\t" \
"v_add_f32 v106, 1.0, v106\n\t" \
"v_rcp_f32 v104, v104\n\t" \
"v_rcp_f32 v106, v106\n\t" \
"v_mul_f32 v110, v104, v105\n\t" \
"v_mul_f32 v111, v106, v107\n\t" \
"v_mul_f32 v100, v88, v121\n\t" \
"v_mul_f32 v102, v89, v121\n\t" \
"v_mul_f32 v101, v96, v123\n\t" \
"v_mul_f32 v103, v97, v123\n\t" \
"v_fmac_f32 v100, v20, v120\n\t" \
"v_fmac_f32 v102, v21, v120\n\t" \
"v_fmac_f32 v101, v28, v122\n\t" \
"v_fmac_f32 v103, v29, v122\n\t" \
"v_exp_f32 v100, v100\n\t" \
"v_exp_f32 v102, v102\n\t" \
"v_add_f32 v100, 1.0, v100\n\t" \
"v_add_f32 v102, 1.0, v102\n\t" \
"v_rcp_f32 v100, v100\n\t" \
"v_rcp_f32 v102, v102\n\t" \
"v_mul_f32 v112, v100, v101\n\t" \
"v_mul_f32 v113, v102, v103\n\t" \
"v_mul_f32 v104, v90, v121\n\t" \
"v_mul_f32 v106, v91, v121\n\t" \
"v_mul_f32 v105, v98, v123\n\t" \
"v_mul_f32 v107, v99, v123\n\t" \
"v_fmac_f32 v104, v22, v120\n\t" \
"v_fmac_f32 v106, v23, v120\n\t" \
"v_fmac_f32 v105, v30, v122\n\t" \
"v_fmac_f32 v107, v31, v122\n\t" \
"v_exp_f32 v104, v104\n\t" \
"v_exp_f32 v106, v106\n\t" \
"v_add_f32 v104, 1.0, v104\n\t" \
"v_add_f32 v106, 1.0, v106\n\t" \
"v_rcp_f32 v104, v104\n\t" \
"v_rcp_f32 v106, v106\n\t" \
"v_mul_f32 v114, v104, v105\n\t" \
"v_mul_f32 v115, v106, v107\n\t" \
"v_cvt_pk_bf16_f32 v116, v108, v109\n\t" \
"v_cvt_pk_bf16_f32 v117, v110, v111\n\t" \
"v_cvt_pk_bf16_f32 v118, v112, v113\n\t" \
"v_cvt_pk_bf16_f32 v119, v114, v115\n\t" \
"buffer_store_dwordx4 v[116:119], v74, %[rs], %[ao] offen offset:1024 sc1\n\t" \
"7:\n\t" \
"s_nop 1\n\t"

#define P5_LOOP \
"buffer_load_dwordx4 v[128:131], v52, %[rs], %[so] offen offset:0 sc1\n\t" \
"buffer_load_dwordx4 v[132:135], v52, %[rs], %[so] offen offset:1024 sc1\n\t" \
"buffer_load_dwordx4 v[136:139], v52, %[rs], %[so] offen offset:2048 sc1\n\t" \
"buffer_load_dwordx4 v[140:143], v52, %[rs], %[so] offen offset:3072 sc1\n\t" \
"ds_read_b64_tr_b16 v[32:33], v48 offset:0\n\t" \
"ds_read_b64_tr_b16 v[34:35], v48 offset:1024\n\t" \
"ds_read_b64_tr_b16 v[36:37], v49 offset:0\n\t" \
"ds_read_b64_tr_b16 v[38:39], v49 offset:1024\n\t" \
"ds_read_b64_tr_b16 v[40:41], v48 offset:8192\n\t" \
"ds_read_b64_tr_b16 v[42:43], v48 offset:9216\n\t" \
"ds_read_b64_tr_b16 v[44:45], v49 offset:8192\n\t" \
"ds_read_b64_tr_b16 v[46:47], v49 offset:9216\n\t" \
"s_waitcnt vmcnt(3) lgkmcnt(6)\n\t" \
"v_mfma_f32_32x32x16_bf16 v[0:15], v[32:35], v[128:131], v[0:15]\n\t" \
"s_add_u32 %[so], %[so], 0x1000\n\t" \
"buffer_load_dwordx4 v[144:147], v52, %[rs], %[so] offen offset:0 sc1\n\t" \
"s_waitcnt lgkmcnt(4)\n\t" \
"v_mfma_f32_32x32x16_bf16 v[16:31], v[36:39], v[128:131], v[16:31]\n\t" \
"buffer_load_dwordx4 v[148:151], v52, %[rs], %[so] offen offset:1024 sc1\n\t" \
"ds_read_b64_tr_b16 v[32:33], v48 offset:16384\n\t" \
"ds_read_b64_tr_b16 v[34:35], v48 offset:17408\n\t" \
"ds_read_b64_tr_b16 v[36:37], v49 offset:16384\n\t" \
"ds_read_b64_tr_b16 v[38:39], v49 offset:17408\n\t" \
"s_waitcnt vmcnt(4) lgkmcnt(6)\n\t" \
"v_mfma_f32_32x32x16_bf16 v[0:15], v[40:43], v[132:135], v[0:15]\n\t" \
"buffer_load_dwordx4 v[152:155], v52, %[rs], %[so] offen offset:2048 sc1\n\t" \
"s_waitcnt lgkmcnt(4)\n\t" \
"v_mfma_f32_32x32x16_bf16 v[16:31], v[44:47], v[132:135], v[16:31]\n\t" \
"buffer_load_dwordx4 v[156:159], v52, %[rs], %[so] offen offset:3072 sc1\n\t" \
"ds_read_b64_tr_b16 v[40:41], v48 offset:24576\n\t" \
"ds_read_b64_tr_b16 v[42:43], v48 offset:25600\n\t" \
"ds_read_b64_tr_b16 v[44:45], v49 offset:24576\n\t" \
"ds_read_b64_tr_b16 v[46:47], v49 offset:25600\n\t" \
"s_waitcnt vmcnt(5) lgkmcnt(6)\n\t" \
"v_mfma_f32_32x32x16_bf16 v[0:15], v[32:35], v[136:139], v[0:15]\n\t" \
"s_add_u32 %[so], %[so], 0x1000\n\t" \
"buffer_load_dwordx4 v[160:163], v52, %[rs], %[so] offen offset:0 sc1\n\t" \
"s_waitcnt lgkmcnt(4)\n\t" \
"v_mfma_f32_32x32x16_bf16 v[16:31], v[36:39], v[136:139], v[16:31]\n\t" \
"buffer_load_dwordx4 v[164:167], v52, %[rs], %[so] offen offset:1024 sc1\n\t" \
"ds_read_b64_tr_b16 v[32:33], v48 offset:32768\n\t" \
"ds_read_b64_tr_b16 v[34:35], v48 offset:33792\n\t" \
"ds_read_b64_tr_b16 v[36:37], v49 offset:32768\n\t" \
"ds_read_b64_tr_b16 v[38:39], v49 offset:33792\n\t" \
"s_barrier\n\t" \
"s_mov_b32 %[keep], m0\n\t" \
"s_mov_b32 m0, %[l0]\n\t" \
"s_nop 0\n\t" \
"global_load_lds_dwordx4 v53, %[g0]\n\t" \
"s_cmp_lg_u32 %[sh], 0\n\t" \
"s_cbranch_scc1 1f\n\t" \
"s_add_u32 m0, m0, 0x2000\n\t" \
"s_nop 0\n\t" \
"global_load_lds_dwordx4 v53, %[g1]\n\t" \
"1:\n\t" \
"s_mov_b32 m0, %[keep]\n\t" \
"s_waitcnt vmcnt(6) lgkmcnt(6)\n\t" \
"v_mfma_f32_32x32x16_bf16 v[0:15], v[40:43], v[140:143], v[0:15]\n\t" \
"buffer_load_dwordx4 v[168:171], v52, %[rs], %[so] offen offset:2048 sc1\n\t" \
"s_waitcnt lgkmcnt(4)\n\t" \
"v_mfma_f32_32x32x16_bf16 v[16:31], v[44:47], v[140:143], v[16:31]\n\t" \
"v_subrev_u32 v48, 0x1a000, v48\n\t" \
"ds_read_b64_tr_b16 v[40:41], v48 offset:0\n\t" \
"ds_read_b64_tr_b16 v[42:43], v48 offset:1024\n\t" \
"v_subrev_u32 v49, 0x1a000, v49\n\t" \
"ds_read_b64_tr_b16 v[44:45], v49 offset:0\n\t" \
"ds_read_b64_tr_b16 v[46:47], v49 offset:1024\n\t" \
"s_waitcnt vmcnt(6) lgkmcnt(6)\n\t" \
"v_mfma_f32_32x32x16_bf16 v[0:15], v[32:35], v[144:147], v[0:15]\n\t" \
"buffer_load_dwordx4 v[172:175], v52, %[rs], %[so] offen offset:3072 sc1\n\t" \
"s_waitcnt lgkmcnt(4)\n\t" \
"v_mfma_f32_32x32x16_bf16 v[16:31], v[36:39], v[144:147], v[16:31]\n\t" \
"ds_read_b64_tr_b16 v[32:33], v48 offset:8192\n\t" \
"ds_read_b64_tr_b16 v[34:35], v48 offset:9216\n\t" \
"ds_read_b64_tr_b16 v[36:37], v49 offset:8192\n\t" \
"ds_read_b64_tr_b16 v[38:39], v49 offset:9216\n\t" \
"s_waitcnt vmcnt(6) lgkmcnt(6)\n\t" \
"v_mfma_f32_32x32x16_bf16 v[0:15], v[40:43], v[148:151], v[0:15]\n\t" \
"s_add_u32 %[so], %[so], 0x1000\n\t" \
"buffer_load_dwordx4 v[176:179], v52, %[rs], %[so] offen offset:0 sc1\n\t" \
"s_waitcnt lgkmcnt(4)\n\t" \
"v_mfma_f32_32x32x16_bf16 v[16:31], v[44:47], v[148:151], v[16:31]\n\t" \
"ds_read_b64_tr_b16 v[40:41], v48 offset:16384\n\t" \
"ds_read_b64_tr_b16 v[42:43], v48 offset:17408\n\t" \
"ds_read_b64_tr_b16 v[44:45], v49 offset:16384\n\t" \
"ds_read_b64_tr_b16 v[46:47], v49 offset:17408\n\t" \
"s_waitcnt vmcnt(6) lgkmcnt(6)\n\t" \
"v_mfma_f32_32x32x16_bf16 v[0:15], v[32:35], v[152:155], v[0:15]\n\t" \
"buffer_load_dwordx4 v[180:183], v52, %[rs], %[so] offen offset:1024 sc1\n\t" \
"s_waitcnt lgkmcnt(4)\n\t" \
"v_mfma_f32_32x32x16_bf16 v[16:31], v[36:39], v[152:155], v[16:31]\n\t" \
"ds_read_b64_tr_b16 v[32:33], v48 offset:24576\n\t" \
"ds_read_b64_tr_b16 v[34:35], v48 offset:25600\n\t" \
"ds_read_b64_tr_b16 v[36:37], v49 offset:24576\n\t" \
"ds_read_b64_tr_b16 v[38:39], v49 offset:25600\n\t" \
"s_waitcnt vmcnt(6) lgkmcnt(6)\n\t" \
"v_mfma_f32_32x32x16_bf16 v[0:15], v[40:43], v[156:159], v[0:15]\n\t" \
"buffer_load_dwordx4 v[184:187], v52, %[rs], %[so] offen offset:2048 sc1\n\t" \
"s_waitcnt lgkmcnt(4)\n\t" \
"v_mfma_f32_32x32x16_bf16 v[16:31], v[44:47], v[156:159], v[16:31]\n\t" \
"ds_read_b64_tr_b16 v[40:41], v48 offset:32768\n\t" \
"ds_read_b64_tr_b16 v[42:43], v48 offset:33792\n\t" \
"ds_read_b64_tr_b16 v[44:45], v49 offset:32768\n\t" \
"ds_read_b64_tr_b16 v[46:47], v49 offset:33792\n\t" \
"s_waitcnt vmcnt(6) lgkmcnt(6)\n\t" \
"v_mfma_f32_32x32x16_bf16 v[0:15], v[32:35], v[160:163], v[0:15]\n\t" \
"buffer_load_dwordx4 v[188:191], v52, %[rs], %[so] offen offset:3072 sc1\n\t" \
"s_waitcnt lgkmcnt(4)\n\t" \
"v_mfma_f32_32x32x16_bf16 v[16:31], v[36:39], v[160:163], v[16:31]\n\t" \
"ds_read_b64_tr_b16 v[32:33], v48 offset:40960\n\t" \
"ds_read_b64_tr_b16 v[34:35], v48 offset:41984\n\t" \
"ds_read_b64_tr_b16 v[36:37], v49 offset:40960\n\t" \
"ds_read_b64_tr_b16 v[38:39], v49 offset:41984\n\t" \
"s_waitcnt vmcnt(6) lgkmcnt(6)\n\t" \
"v_mfma_f32_32x32x16_bf16 v[0:15], v[40:43], v[164:167], v[0:15]\n\t" \
"s_add_u32 %[so], %[so], 0x1000\n\t" \
"buffer_load_dwordx4 v[192:195], v52, %[rs], %[so] offen offset:0 sc1\n\t" \
"s_waitcnt lgkmcnt(4)\n\t" \
"v_mfma_f32_32x32x16_bf16 v[16:31], v[44:47], v[164:167], v[16:31]\n\t" \
"ds_read_b64_tr_b16 v[40:41], v48 offset:49152\n\t" \
"ds_read_b64_tr_b16 v[42:43], v48 offset:50176\n\t" \
"ds_read_b64_tr_b16 v[44:45], v49 offset:49152\n\t" \
"ds_read_b64_tr_b16 v[46:47], v49 offset:50176\n\t" \
"s_waitcnt vmcnt(6) lgkmcnt(6)\n\t" \
"v_mfma_f32_32x32x16_bf16 v[0:15], v[32:35], v[168:171], v[0:15]\n\t" \
"buffer_load_dwordx4 v[196:199], v52, %[rs], %[so] offen offset:1024 sc1\n\t" \
"s_waitcnt lgkmcnt(4)\n\t" \
"v_mfma_f32_32x32x16_bf16 v[16:31], v[36:39], v[168:171], v[16:31]\n\t" \
"ds_read_b64_tr_b16 v[32:33], v48 offset:57344\n\t" \
"ds_read_b64_tr_b16 v[34:35], v48 offset:58368\n\t" \
"ds_read_b64_tr_b16 v[36:37], v49 offset:57344\n\t" \
"ds_read_b64_tr_b16 v[38:39], v49 offset:58368\n\t" \
"s_waitcnt vmcnt(6) lgkmcnt(6)\n\t" \
"v_mfma_f32_32x32x16_bf16 v[0:15], v[40:43], v[172:175], v[0:15]\n\t" \
"buffer_load_dwordx4 v[200:203], v52, %[rs], %[so] offen offset:2048 sc1\n\t" \
"s_waitcnt lgkmcnt(4)\n\t" \
"v_mfma_f32_32x32x16_bf16 v[16:31], v[44:47], v[172:175], v[16:31]\n\t" \
"v_add_u32 v48, 0x10000, v48\n\t" \
"ds_read_b64_tr_b16 v[40:41], v48 offset:0\n\t" \
"ds_read_b64_tr_b16 v[42:43], v48 offset:1024\n\t" \
"v_add_u32 v49, 0x10000, v49\n\t" \
"ds_read_b64_tr_b16 v[44:45], v49 offset:0\n\t" \
"ds_read_b64_tr_b16 v[46:47], v49 offset:1024\n\t" \
"s_waitcnt vmcnt(6) lgkmcnt(6)\n\t" \
"v_mfma_f32_32x32x16_bf16 v[0:15], v[32:35], v[176:179], v[0:15]\n\t" \
"buffer_load_dwordx4 v[204:207], v52, %[rs], %[so] offen offset:3072 sc1\n\t" \
"s_waitcnt lgkmcnt(4)\n\t" \
"v_mfma_f32_32x32x16_bf16 v[16:31], v[36:39], v[176:179], v[16:31]\n\t" \
"ds_read_b64_tr_b16 v[32:33], v48 offset:8192\n\t" \
"ds_read_b64_tr_b16 v[34:35], v48 offset:9216\n\t" \
"ds_read_b64_tr_b16 v[36:37], v49 offset:8192\n\t" \
"ds_read_b64_tr_b16 v[38:39], v49 offset:9216\n\t" \
"s_waitcnt vmcnt(6) lgkmcnt(6)\n\t" \
"v_mfma_f32_32x32x16_bf16 v[0:15], v[40:43], v[180:183], v[0:15]\n\t" \
"s_add_u32 %[so], %[so], 0x1000\n\t" \
"buffer_load_dwordx4 v[208:211], v52, %[rs], %[so] offen offset:0 sc1\n\t" \
"s_waitcnt lgkmcnt(4)\n\t" \
"v_mfma_f32_32x32x16_bf16 v[16:31], v[44:47], v[180:183], v[16:31]\n\t" \
"ds_read_b64_tr_b16 v[40:41], v48 offset:16384\n\t" \
"ds_read_b64_tr_b16 v[42:43], v48 offset:17408\n\t" \
"ds_read_b64_tr_b16 v[44:45], v49 offset:16384\n\t" \
"ds_read_b64_tr_b16 v[46:47], v49 offset:17408\n\t" \
"s_waitcnt vmcnt(6) lgkmcnt(6)\n\t" \
"v_mfma_f32_32x32x16_bf16 v[0:15], v[32:35], v[184:187], v[0:15]\n\t" \
"s_sub_u32 %[so], %[so], %[sh]\n\t" \
"buffer_load_dwordx4 v[212:215], v52, %[rs], %[so] offen offset:1024 sc1\n\t" \
"s_waitcnt lgkmcnt(4)\n\t" \
"v_mfma_f32_32x32x16_bf16 v[16:31], v[36:39], v[184:187], v[16:31]\n\t" \
"ds_read_b64_tr_b16 v[32:33], v48 offset:24576\n\t" \
"ds_read_b64_tr_b16 v[34:35], v48 offset:25600\n\t" \
"ds_read_b64_tr_b16 v[36:37], v49 offset:24576\n\t" \
"ds_read_b64_tr_b16 v[38:39], v49 offset:25600\n\t" \
"s_waitcnt vmcnt(6) lgkmcnt(6)\n\t" \
"v_mfma_f32_32x32x16_bf16 v[0:15], v[40:43], v[188:191], v[0:15]\n\t" \
"s_waitcnt lgkmcnt(4)\n\t" \
"v_mfma_f32_32x32x16_bf16 v[16:31], v[44:47], v[188:191], v[16:31]\n\t" \
"ds_read_b64_tr_b16 v[40:41], v48 offset:32768\n\t" \
"ds_read_b64_tr_b16 v[42:43], v48 offset:33792\n\t" \
"ds_read_b64_tr_b16 v[44:45], v49 offset:32768\n\t" \
"ds_read_b64_tr_b16 v[46:47], v49 offset:33792\n\t" \
"s_waitcnt vmcnt(5) lgkmcnt(6)\n\t" \
"v_mfma_f32_32x32x16_bf16 v[0:15], v[32:35], v[192:195], v[0:15]\n\t" \
"s_waitcnt lgkmcnt(4)\n\t" \
"v_mfma_f32_32x32x16_bf16 v[16:31], v[36:39], v[192:195], v[16:31]\n\t" \
"v_add_u32 v48, 0x14000, v48\n\t" \
"ds_read_b64_tr_b16 v[32:33], v48 offset:0\n\t" \
"ds_read_b64_tr_b16 v[34:35], v48 offset:1024\n\t" \
"v_add_u32 v49, 0x14000, v49\n\t" \
"ds_read_b64_tr_b16 v[36:37], v49 offset:0\n\t" \
"ds_read_b64_tr_b16 v[38:39], v49 offset:1024\n\t" \
"s_waitcnt vmcnt(4) lgkmcnt(6)\n\t" \
"v_mfma_f32_32x32x16_bf16 v[0:15], v[40:43], v[196:199], v[0:15]\n\t" \
"s_waitcnt lgkmcnt(4)\n\t" \
"v_mfma_f32_32x32x16_bf16 v[16:31], v[44:47], v[196:199], v[16:31]\n\t" \
"ds_read_b64_tr_b16 v[40:41], v48 offset:8192\n\t" \
"ds_read_b64_tr_b16 v[42:43], v48 offset:9216\n\t" \
"ds_read_b64_tr_b16 v[44:45], v49 offset:8192\n\t" \
"ds_read_b64_tr_b16 v[46:47], v49 offset:9216\n\t" \
"s_waitcnt vmcnt(3) lgkmcnt(6)\n\t" \
"v_mfma_f32_32x32x16_bf16 v[0:15], v[32:35], v[200:203], v[0:15]\n\t" \
"s_waitcnt lgkmcnt(4)\n\t" \
"v_mfma_f32_32x32x16_bf16 v[16:31], v[36:39], v[200:203], v[16:31]\n\t" \
"s_waitcnt vmcnt(2) lgkmcnt(2)\n\t" \
"v_mfma_f32_32x32x16_bf16 v[0:15], v[40:43], v[204:207], v[0:15]\n\t" \
"s_waitcnt lgkmcnt(0)\n\t" \
"v_mfma_f32_32x32x16_bf16 v[16:31], v[44:47], v[204:207], v[16:31]\n\t" \
"s_waitcnt vmcnt(0)\n\t" \
"s_barrier\n\t" \
"v_subrev_u32 v48, 0xa000, v48\n\t" \
"ds_read_b64_tr_b16 v[32:33], v48 offset:0\n\t" \
"ds_read_b64_tr_b16 v[34:35], v48 offset:1024\n\t" \
"v_subrev_u32 v49, 0xa000, v49\n\t" \
"ds_read_b64_tr_b16 v[36:37], v49 offset:0\n\t" \
"ds_read_b64_tr_b16 v[38:39], v49 offset:1024\n\t" \
"ds_read_b64_tr_b16 v[40:41], v48 offset:8192\n\t" \
"ds_read_b64_tr_b16 v[42:43], v48 offset:9216\n\t" \
"ds_read_b64_tr_b16 v[44:45], v49 offset:8192\n\t" \
"ds_read_b64_tr_b16 v[46:47], v49 offset:9216\n\t" \
"s_waitcnt vmcnt(1) lgkmcnt(6)\n\t" \
"v_mfma_f32_32x32x16_bf16 v[0:15], v[32:35], v[208:211], v[0:15]\n\t" \
"s_waitcnt lgkmcnt(4)\n\t" \
"v_mfma_f32_32x32x16_bf16 v[16:31], v[36:39], v[208:211], v[16:31]\n\t" \
"s_cmp_lg_u32 %[sh], 0\n\t" \
"s_cbranch_scc1 2f\n\t" \
"s_waitcnt vmcnt(0) lgkmcnt(2)\n\t" \
"v_mfma_f32_32x32x16_bf16 v[0:15], v[40:43], v[212:215], v[0:15]\n\t" \
"s_waitcnt lgkmcnt(0)\n\t" \
"v_mfma_f32_32x32x16_bf16 v[16:31], v[44:47], v[212:215], v[16:31]\n\t" \
"2:\n\t" \
"s_waitcnt lgkmcnt(0)\n\t" \
"s_nop 11\n\t"

#define COLSUM \
"ds_read_b64_tr_b16 v[20:21], v48 offset:0\n\t" \
"ds_read_b64_tr_b16 v[22:23], v48 offset:768\n\t" \
"ds_read_b64_tr_b16 v[24:25], v48 offset:1536\n\t" \
"ds_read_b64_tr_b16 v[26:27], v48 offset:2304\n\t" \
"ds_read_b64_tr_b16 v[28:29], v48 offset:3072\n\t" \
"ds_read_b64_tr_b16 v[30:31], v48 offset:3840\n\t" \
"ds_read_b64_tr_b16 v[32:33], v48 offset:4608\n\t" \
"ds_read_b64_tr_b16 v[34:35], v48 offset:5376\n\t" \
"ds_read_b64_tr_b16 v[36:37], v48 offset:6144\n\t" \
"ds_read_b64_tr_b16 v[38:39], v48 offset:6912\n\t" \
"ds_read_b64_tr_b16 v[40:41], v48 offset:7680\n\t" \
"ds_read_b64_tr_b16 v[42:43], v48 offset:8448\n\t" \
"ds_read_b64_tr_b16 v[44:45], v48 offset:9216\n\t" \
"ds_read_b64_tr_b16 v[46:47], v48 offset:9984\n\t" \
"s_waitcnt lgkmcnt(12)\n\t" \
"v_mfma_f32_32x32x16_bf16 v[0:15], v[20:23], v[16:19], 0\n\t" \
"ds_read_b64_tr_b16 v[20:21], v48 offset:10752\n\t" \
"ds_read_b64_tr_b16 v[22:23], v48 offset:11520\n\t" \
"s_waitcnt lgkmcnt(12)\n\t" \
"v_mfma_f32_32x32x16_bf16 v[0:15], v[24:27], v[16:19], v[0:15]\n\t" \
"ds_read_b64_tr_b16 v[24:25], v48 offset:12288\n\t" \
"ds_read_b64_tr_b16 v[26:27], v48 offset:13056\n\t" \
"s_waitcnt lgkmcnt(12)\n\t" \
"v_mfma_f32_32x32x16_bf16 v[0:15], v[28:31], v[16:19], v[0:15]\n\t" \
"ds_read_b64_tr_b16 v[28:29], v48 offset:13824\n\t" \
"ds_read_b64_tr_b16 v[30:31], v48 offset:14592\n\t" \
"s_waitcnt lgkmcnt(12)\n\t" \
"v_mfma_f32_32x32x16_bf16 v[0:15], v[32:35], v[16:19], v[0:15]\n\t" \
"ds_read_b64_tr_b16 v[32:33], v48 offset:15360\n\t" \
"ds_read_b64_tr_b16 v[34:35], v48 offset:16128\n\t" \
"s_waitcnt lgkmcnt(12)\n\t" \
"v_mfma_f32_32x32x16_bf16 v[0:15], v[36:39], v[16:19], v[0:15]\n\t" \
"ds_read_b64_tr_b16 v[36:37], v48 offset:16896\n\t" \
"ds_read_b64_tr_b16 v[38:39], v48 offset:17664\n\t" \
"s_waitcnt lgkmcnt(12)\n\t" \
"v_mfma_f32_32x32x16_bf16 v[0:15], v[40:43], v[16:19], v[0:15]\n\t" \
"ds_read_b64_tr_b16 v[40:41], v48 offset:18432\n\t" \
"ds_read_b64_tr_b16 v[42:43], v48 offset:19200\n\t" \
"s_waitcnt lgkmcnt(12)\n\t" \
"v_mfma_f32_32x32x16_bf16 v[0:15], v[44:47], v[16:19], v[0:15]\n\t" \
"ds_read_b64_tr_b16 v[44:45], v48 offset:19968\n\t" \
"ds_read_b64_tr_b16 v[46:47], v48 offset:20736\n\t" \
"s_waitcnt lgkmcnt(12)\n\t" \
"v_mfma_f32_32x32x16_bf16 v[0:15], v[20:23], v[16:19], v[0:15]\n\t" \
"ds_read_b64_tr_b16 v[20:21], v48 offset:21504\n\t" \
"ds_read_b64_tr_b16 v[22:23], v48 offset:22272\n\t" \
"s_waitcnt lgkmcnt(12)\n\t" \
"v_mfma_f32_32x32x16_bf16 v[0:15], v[24:27], v[16:19], v[0:15]\n\t" \
"ds_read_b64_tr_b16 v[24:25], v48 offset:23040\n\t" \
"ds_read_b64_tr_b16 v[26:27], v48 offset:23808\n\t" \
"s_waitcnt lgkmcnt(12)\n\t" \
"v_mfma_f32_32x32x16_bf16 v[0:15], v[28:31], v[16:19], v[0:15]\n\t" \
"ds_read_b64_tr_b16 v[28:29], v48 offset:24576\n\t" \
"ds_read_b64_tr_b16 v[30:31], v48 offset:25344\n\t" \
"s_waitcnt lgkmcnt(12)\n\t" \
"v_mfma_f32_32x32x16_bf16 v[0:15], v[32:35], v[16:19], v[0:15]\n\t" \
"ds_read_b64_tr_b16 v[32:33], v48 offset:26112\n\t" \
"ds_read_b64_tr_b16 v[34:35], v48 offset:26880\n\t" \
"s_waitcnt lgkmcnt(12)\n\t" \
"v_mfma_f32_32x32x16_bf16 v[0:15], v[36:39], v[16:19], v[0:15]\n\t" \
"ds_read_b64_tr_b16 v[36:37], v48 offset:27648\n\t" \
"ds_read_b64_tr_b16 v[38:39], v48 offset:28416\n\t" \
"s_waitcnt lgkmcnt(12)\n\t" \
"v_mfma_f32_32x32x16_bf16 v[0:15], v[40:43], v[16:19], v[0:15]\n\t" \
"ds_read_b64_tr_b16 v[40:41], v48 offset:29184\n\t" \
"ds_read_b64_tr_b16 v[42:43], v48 offset:29952\n\t" \
"s_waitcnt lgkmcnt(12)\n\t" \
"v_mfma_f32_32x32x16_bf16 v[0:15], v[44:47], v[16:19], v[0:15]\n\t" \
"ds_read_b64_tr_b16 v[44:45], v48 offset:30720\n\t" \
"ds_read_b64_tr_b16 v[46:47], v48 offset:31488\n\t" \
"s_waitcnt lgkmcnt(12)\n\t" \
"v_mfma_f32_32x32x16_bf16 v[0:15], v[20:23], v[16:19], v[0:15]\n\t" \
"ds_read_b64_tr_b16 v[20:21], v48 offset:32256\n\t" \
"ds_read_b64_tr_b16 v[22:23], v48 offset:33024\n\t" \
"s_waitcnt lgkmcnt(12)\n\t" \
"v_mfma_f32_32x32x16_bf16 v[0:15], v[24:27], v[16:19], v[0:15]\n\t" \
"ds_read_b64_tr_b16 v[24:25], v48 offset:33792\n\t" \
"ds_read_b64_tr_b16 v[26:27], v48 offset:34560\n\t" \
"s_waitcnt lgkmcnt(12)\n\t" \
"v_mfma_f32_32x32x16_bf16 v[0:15], v[28:31], v[16:19], v[0:15]\n\t" \
"ds_read_b64_tr_b16 v[28:29], v48 offset:35328\n\t" \
"ds_read_b64_tr_b16 v[30:31], v48 offset:36096\n\t" \
"s_waitcnt lgkmcnt(12)\n\t" \
"v_mfma_f32_32x32x16_bf16 v[0:15], v[32:35], v[16:19], v[0:15]\n\t" \
"ds_read_b64_tr_b16 v[32:33], v48 offset:36864\n\t" \
"ds_read_b64_tr_b16 v[34:35], v48 offset:37632\n\t" \
"s_waitcnt lgkmcnt(12)\n\t" \
"v_mfma_f32_32x32x16_bf16 v[0:15], v[36:39], v[16:19], v[0:15]\n\t" \
"ds_read_b64_tr_b16 v[36:37], v48 offset:38400\n\t" \
"ds_read_b64_tr_b16 v[38:39], v48 offset:39168\n\t" \
"s_waitcnt lgkmcnt(12)\n\t" \
"v_mfma_f32_32x32x16_bf16 v[0:15], v[40:43], v[16:19], v[0:15]\n\t" \
"ds_read_b64_tr_b16 v[40:41], v48 offset:39936\n\t" \
"ds_read_b64_tr_b16 v[42:43], v48 offset:40704\n\t" \
"s_waitcnt lgkmcnt(12)\n\t" \
"v_mfma_f32_32x32x16_bf16 v[0:15], v[44:47], v[16:19], v[0:15]\n\t" \
"ds_read_b64_tr_b16 v[44:45], v48 offset:41472\n\t" \
"ds_read_b64_tr_b16 v[46:47], v48 offset:42240\n\t" \
"s_waitcnt lgkmcnt(12)\n\t" \
"v_mfma_f32_32x32x16_bf16 v[0:15], v[20:23], v[16:19], v[0:15]\n\t" \
"ds_read_b64_tr_b16 v[20:21], v48 offset:43008\n\t" \
"ds_read_b64_tr_b16 v[22:23], v48 offset:43776\n\t" \
"s_waitcnt lgkmcnt(12)\n\t" \
"v_mfma_f32_32x32x16_bf16 v[0:15], v[24:27], v[16:19], v[0:15]\n\t" \
"ds_read_b64_tr_b16 v[24:25], v48 offset:44544\n\t" \
"ds_read_b64_tr_b16 v[26:27], v48 offset:45312\n\t" \
"s_waitcnt lgkmcnt(12)\n\t" \
"v_mfma_f32_32x32x16_bf16 v[0:15], v[28:31], v[16:19], v[0:15]\n\t" \
"ds_read_b64_tr_b16 v[28:29], v48 offset:46080\n\t" \
"ds_read_b64_tr_b16 v[30:31], v48 offset:46848\n\t" \
"s_waitcnt lgkmcnt(12)\n\t" \
"v_mfma_f32_32x32x16_bf16 v[0:15], v[32:35], v[16:19], v[0:15]\n\t" \
"ds_read_b64_tr_b16 v[32:33], v48 offset:47616\n\t" \
"ds_read_b64_tr_b16 v[34:35], v48 offset:48384\n\t" \
"s_waitcnt lgkmcnt(12)\n\t" \
"v_mfma_f32_32x32x16_bf16 v[0:15], v[36:39], v[16:19], v[0:15]\n\t" \
"s_waitcnt lgkmcnt(10)\n\t" \
"v_mfma_f32_32x32x16_bf16 v[0:15], v[40:43], v[16:19], v[0:15]\n\t" \
"s_waitcnt lgkmcnt(8)\n\t" \
"v_mfma_f32_32x32x16_bf16 v[0:15], v[44:47], v[16:19], v[0:15]\n\t" \
"s_waitcnt lgkmcnt(6)\n\t" \
"v_mfma_f32_32x32x16_bf16 v[0:15], v[20:23], v[16:19], v[0:15]\n\t" \
"s_waitcnt lgkmcnt(4)\n\t" \
"v_mfma_f32_32x32x16_bf16 v[0:15], v[24:27], v[16:19], v[0:15]\n\t" \
"s_waitcnt lgkmcnt(2)\n\t" \
"v_mfma_f32_32x32x16_bf16 v[0:15], v[28:31], v[16:19], v[0:15]\n\t" \
"s_waitcnt lgkmcnt(0)\n\t" \
"v_mfma_f32_32x32x16_bf16 v[0:15], v[32:35], v[16:19], v[0:15]\n\t" \
"s_nop 11\n\t"

#define CLOB_128_167 "v128", "v129", "v130", "v131", "v132", "v133", "v134", "v135", "v136", "v137", "v138", "v139", "v140", "v141", "v142", "v143", "v144", "v145", "v146", "v147", "v148", "v149", "v150", "v151", "v152", "v153", "v154", "v155", "v156", "v157", "v158", "v159", "v160", "v161", "v162", "v163", "v164", "v165", "v166", "v167"
#define CLOB_112_151 "v112", "v113", "v114", "v115", "v116", "v117", "v118", "v119", "v120", "v121", "v122", "v123", "v124", "v125", "v126", "v127", "v128", "v129", "v130", "v131", "v132", "v133", "v134", "v135", "v136", "v137", "v138", "v139", "v140", "v141", "v142", "v143", "v144", "v145", "v146", "v147", "v148", "v149", "v150", "v151"
#define ZERO_STATE "v_mov_b32 v100, 0xdddd\n\tv_mov_b32 v119, 0\n\tv_mov_b32 v120, 0\n\tv_mov_b64 v[0:1], 0\n\tv_mov_b64 v[2:3], 0\n\tv_mov_b64 v[4:5], 0\n\tv_mov_b64 v[6:7], 0\n\tv_mov_b64 v[8:9], 0\n\tv_mov_b64 v[10:11], 0\n\tv_mov_b64 v[12:13], 0\n\tv_mov_b64 v[14:15], 0\n\tv_mov_b64 v[16:17], 0\n\tv_mov_b64 v[18:19], 0\n\tv_mov_b64 v[20:21], 0\n\tv_mov_b64 v[22:23], 0\n\tv_mov_b64 v[24:25], 0\n\tv_mov_b64 v[26:27], 0\n\tv_mov_b64 v[28:29], 0\n\tv_mov_b64 v[30:31], 0\n\tv_mov_b64 v[72:73], 0\n\tv_mov_b64 v[74:75], 0\n\tv_mov_b64 v[76:77], 0\n\tv_mov_b64 v[78:79], 0\n\tv_mov_b64 v[96:97], 0\n\tv_mov_b64 v[98:99], 0\n\t"
#define CLOB_8_135 "v8", "v9", "v10", "v11", "v12", "v13", "v14", "v15", "v16", "v17", "v18", "v19", "v20", "v21", "v22", "v23", "v24", "v25", "v26", "v27", "v28", "v29", "v30", "v31", "v32", "v33", "v34", "v35", "v36", "v37", "v38", "v39", "v40", "v41", "v42", "v43", "v44", "v45", "v46", "v47", "v48", "v49", "v50", "v51", "v52", "v53", "v54", "v55", "v56", "v57", "v58", "v59", "v60", "v61", "v62", "v63", "v64", "v65", "v66", "v67", "v68", "v69", "v70", "v71", "v72", "v73", "v74", "v75", "v76", "v77", "v78", "v79", "v80", "v81", "v82", "v83", "v84", "v85", "v86", "v87", "v88", "v89", "v90", "v91", "v92", "v93", "v94", "v95", "v96", "v97", "v98", "v99", "v100", "v101", "v102", "v103", "v104", "v105", "v106", "v107", "v108", "v109", "v110", "v111", "v112", "v113", "v114", "v115", "v116", "v117", "v118", "v119", "v120", "v121", "v122", "v123", "v124", "v125", "v126", "v127", "v128", "v129", "v130", "v131", "v132", "v133", "v134", "v135"
#define CLOB_0_71 "v0", "v1", "v2", "v3", "v4", "v5", "v6", "v7", "v8", "v9", "v10", "v11", "v12", "v13", "v14", "v15", "v16", "v17", "v18", "v19", "v20", "v21", "v22", "v23", "v24", "v25", "v26", "v27", "v28", "v29", "v30", "v31", "v32", "v33", "v34", "v35", "v36", "v37", "v38", "v39", "v40", "v41", "v42", "v43", "v44", "v45", "v46", "v47", "v48", "v49", "v50", "v51", "v52", "v53", "v54", "v55", "v56", "v57", "v58", "v59", "v60", "v61", "v62", "v63", "v64", "v65", "v66", "v67", "v68", "v69", "v70", "v71"
#define CLOB_76_123 "v76", "v77", "v78", "v79", "v80", "v81", "v82", "v83", "v84", "v85", "v86", "v87", "v88", "v89", "v90", "v91", "v92", "v93", "v94", "v95", "v96", "v97", "v98", "v99", "v100", "v101", "v102", "v103", "v104", "v105", "v106", "v107", "v108", "v109", "v110", "v111", "v112", "v113", "v114", "v115", "v116", "v117", "v118", "v119", "v120", "v121", "v122", "v123"
#define CLOB_8_103 "v8", "v9", "v10", "v11", "v12", "v13", "v14", "v15", "v16", "v17", "v18", "v19", "v20", "v21", "v22", "v23", "v24", "v25", "v26", "v27", "v28", "v29", "v30", "v31", "v32", "v33", "v34", "v35", "v36", "v37", "v38", "v39", "v40", "v41", "v42", "v43", "v44", "v45", "v46", "v47", "v48", "v49", "v50", "v51", "v52", "v53", "v54", "v55", "v56", "v57", "v58", "v59", "v60", "v61", "v62", "v63", "v64", "v65", "v66", "v67", "v68", "v69", "v70", "v71", "v72", "v73", "v74", "v75", "v76", "v77", "v78", "v79", "v80", "v81", "v82", "v83", "v84", "v85", "v86", "v87", "v88", "v89", "v90", "v91", "v92", "v93", "v94", "v95", "v96", "v97", "v98", "v99", "v100", "v101", "v102", "v103"
#define CLOB_188_243 "v188", "v189", "v190", "v191", "v192", "v193", "v194", "v195", "v196", "v197", "v198", "v199", "v200", "v201", "v202", "v203", "v204", "v205", "v206", "v207", "v208", "v209", "v210", "v211", "v212", "v213", "v214", "v215", "v216", "v217", "v218", "v219", "v220", "v221", "v222", "v223", "v224", "v225", "v226", "v227", "v228", "v229", "v230", "v231", "v232", "v233", "v234", "v235", "v236", "v237", "v238", "v239", "v240", "v241", "v242", "v243"
#define CLOB_40_79 "v40", "v41", "v42", "v43", "v44", "v45", "v46", "v47", "v48", "v49", "v50", "v51", "v52", "v53", "v54", "v55", "v56", "v57", "v58", "v59", "v60", "v61", "v62", "v63", "v64", "v65", "v66", "v67", "v68", "v69", "v70", "v71", "v72", "v73", "v74", "v75", "v76", "v77", "v78", "v79"
#define CLOB_16_79 "v16", "v17", "v18", "v19", "v20", "v21", "v22", "v23", "v24", "v25", "v26", "v27", "v28", "v29", "v30", "v31", "v32", "v33", "v34", "v35", "v36", "v37", "v38", "v39", "v40", "v41", "v42", "v43", "v44", "v45", "v46", "v47", "v48", "v49", "v50", "v51", "v52", "v53", "v54", "v55", "v56", "v57", "v58", "v59", "v60", "v61", "v62", "v63", "v64", "v65", "v66", "v67", "v68", "v69", "v70", "v71", "v72", "v73", "v74", "v75", "v76", "v77", "v78", "v79"
#define CLOB_128_255 "v128", "v129", "v130", "v131", "v132", "v133", "v134", "v135", "v136", "v137", "v138", "v139", "v140", "v141", "v142", "v143", "v144", "v145", "v146", "v147", "v148", "v149", "v150", "v151", "v152", "v153", "v154", "v155", "v156", "v157", "v158", "v159", "v160", "v161", "v162", "v163", "v164", "v165", "v166", "v167", "v168", "v169", "v170", "v171", "v172", "v173", "v174", "v175", "v176", "v177", "v178", "v179", "v180", "v181", "v182", "v183", "v184", "v185", "v186", "v187", "v188", "v189", "v190", "v191", "v192", "v193", "v194", "v195", "v196", "v197", "v198", "v199", "v200", "v201", "v202", "v203", "v204", "v205", "v206", "v207", "v208", "v209", "v210", "v211", "v212", "v213", "v214", "v215", "v216", "v217", "v218", "v219", "v220", "v221", "v222", "v223", "v224", "v225", "v226", "v227", "v228", "v229", "v230", "v231", "v232", "v233", "v234", "v235", "v236", "v237", "v238", "v239", "v240", "v241", "v242", "v243", "v244", "v245", "v246", "v247", "v248", "v249", "v250", "v251", "v252", "v253", "v254", "v255"

DI void dma16(unsigned voff, const void* sbase, unsigned lds) {
  unsigned keep;
  asm volatile("s_mov_b32 %0, m0\n\ts_mov_b32 m0, %3\n\ts_nop 0\n\tglobal_load_lds_dwordx4 %1, %2\n\ts_mov_b32 m0, %0"
               : "=&s"(keep) : "v"(voff), "s"(sbase), "s"(lds) : "memory");
}
DI void dma16_sc1(unsigned voff, const void* sbase, unsigned lds) {
  unsigned keep;
  asm volatile("s_mov_b32 %0, m0\n\ts_mov_b32 m0, %3\n\ts_nop 0\n\tglobal_load_lds_dwordx4 %1, %2 sc1\n\ts_mov_b32 m0, %0"
               : "=&s"(keep) : "v"(voff), "s"(sbase), "s"(lds) : "memory");
}
typedef short v4i16_t __attribute__((ext_vector_type(4)));
DI s16x4 trd(lptr p) { return __builtin_bit_cast(s16x4, __builtin_amdgcn_ds_read_tr16_b64_v4i16((LAS v4i16_t*)p)); }
DI bf16x8 trd2(lptr p0, lptr p1) { const s16x4 a = trd(p0), b = trd(p1); return __builtin_bit_cast(bf16x8, __builtin_shufflevector(a, b, 0, 1, 2, 3, 4, 5, 6, 7)); }
DI unsigned pk(float lo, float hi) { f32x2 v = {lo, hi}; return __builtin_bit_cast(unsigned, __builtin_convertvector(v, bf16x2)); }
DI float bflo(unsigned u) { return __uint_as_float(u << 16); }
DI float bfhi(unsigned u) { return __uint_as_float(u & 0xffff0000u); }
DI bf16x8 ones8() { const u32x4 o = {0x3f803f80u, 0x3f803f80u, 0x3f803f80u, 0x3f803f80u}; return __builtin_bit_cast(bf16x8, o); }
template <int I> DI f32x4 q4(f32x16 v) { return __builtin_shufflevector(v, v, 4 * I, 4 * I + 1, 4 * I + 2, 4 * I + 3); }

DI void publish(char* ws, int ho, int grp, int slot, u64 tag, int lane) {
  if (lane == 0) __hip_atomic_store((gu64*)(ws + WS_FLAG + ((ho * 8 + grp) * 32 + slot) * FSTR), tag, RLX_AGENT);
}
DI void poll(char* ws, int ho, int grp, u64 tag, int lane) {
  gu64* f = (gu64*)(ws + WS_FLAG + ((ho * 8 + grp) * 32 + (lane & 31)) * FSTR);
#pragma unroll 1
  for (unsigned spin = 0; spin < (1u << 18); ++spin) {
    if (__all(__hip_atomic_load(f, RLX_AGENT) == tag)) break;
    __builtin_amdgcn_s_sleep(1);
  }
  __builtin_amdgcn_fence(__ATOMIC_ACQUIRE, "wavefront");
}

__global__ __launch_bounds__(512, 2) void fused_block(const u16* __restrict__ x, const u16* __restrict__ wqkv, const u16* __restrict__ wo, const u16* __restrict__ wg,
                                                      const u16* __restrict__ wu, const u16* __restrict__ wd, u16* __restrict__ out, char* __restrict__ ws) {
  extern __shared__ __attribute__((aligned(16))) char smem[];
  const int b = blockIdx.x, xq = b & 7, j = b >> 3;
  const int w = __builtin_amdgcn_readfirstlane(threadIdx.x >> 6);
#define LANES int lane; asm volatile("v_mbcnt_lo_u32_b32 %0, -1, 0\n\tv_mbcnt_hi_u32_b32 %0, -1, %0" : "=v"(lane)); \
  const int t = lane & 15, g = lane >> 4, q = t >> 2, p = lane & 3, r = lane & 31, h = lane >> 5; (void)t; (void)g; (void)q; (void)p; (void)r; (void)h
  const unsigned lds0 = (unsigned)(size_t)(lptr)smem;
  const auto wsr = __builtin_amdgcn_make_buffer_rsrc(ws, 0, (unsigned)WS_END, 0x00020000);
  const u32x4 wsd = {(unsigned)(size_t)ws, (unsigned)((size_t)ws >> 32) & 0xffffu, (unsigned)WS_END, 0x00020000u};
  const int P3T = xq * 8 + (j >> 2), P3C = j & 3;
  u64 tag; unsigned pfc, pft;
  u32x32 wo0, wo1;
  {
    LANES;
    {
      u64 pc; unsigned len;
      asm volatile("s_getpc_b64 %0\n.Lfb_begin:\n\ts_mov_b32 %1, .Lfb_end-.Lfb_begin" : "=s"(pc), "=s"(len));
      asm volatile("global_load_dword %0, %1, %2" : "=v"(pfc) : "v"(min((unsigned)(j * 8 + w) * 128u, len) & ~3u), "s"(pc) : "memory");
    }
    const int tt = (xq >> 1) * 8 + (j >> 2), nq = (xq & 1) * 4 + (j & 3), m = w & 1, hl = w >> 1;
    unsigned dvx = (unsigned)((tt * 64 + 8 * w + (lane >> 3)) * 1024 + (((lane & 7) ^ ((4 * w + g) & 7)) * 16));
    unsigned dvw = (unsigned)((8 * w + (lane >> 3)) * 3072 + nq * 128 + (((lane & 7) ^ (((g & 1) | ((w & 1) << 1)) << 1)) * 16));
    const char* gbo = (const char*)wo + P3C * 256;
    const u32x4 rx = {(unsigned)(size_t)x, (unsigned)((size_t)x >> 32) & 0xffffu, (unsigned)(S * D * 2), 0x00020000u};
    const u32x4 rw = {(unsigned)((size_t)wqkv - 1024), (unsigned)(((size_t)wqkv - 1024) >> 32) & 0xffffu, (unsigned)(D * 3 * D * 2 + 1024), 0x00020000u};
    unsigned kc = ((j >> 3) * 2 + (j & 1) * 4 + (xq & 1)) & 7;
    const unsigned lw = lds0 + w * 4096; unsigned keep;
    unsigned kx, kw;
    asm volatile(P1_HEAD : "+{s88}"(kc), "={s89}"(kx), "={s90}"(kw), [keep] "=&s"(keep) : "{v92}"(dvx), "{v93}"(dvw), [lw] "s"(lw), "{s[80:83]}"(rx), "{s[84:87]}"(rw) : "memory", "scc");
    unsigned xa0 = lds0 + m * 16384 + (t >> 3) * 4096 + (t & 7) * 128 + ((g ^ (t >> 1)) * 16), xa1 = xa0 ^ 64;
    unsigned wa = lds0 + g * 4096 + q * 128 + ((hl ^ (((q >> 1) & 1) | ((g & 1) << 1))) * 32) + p * 8;
    f32x16 A, B; f32x4 C; f32x8 SX, SQ;
    asm volatile("global_load_dwordx2 %0, %1, %2 sc1" : "=v"(tag) : "v"(0), "s"(ws + WS_EPOCH) : "memory");
    {
      const char* b14 = (const char*)(w < 3 ? (w == 1 ? wg : wu) : (w == 3 ? wd : out));
      const unsigned c57 = w == 5 ? (unsigned)WS_STAT + P3T * 1024 : w == 6 ? (unsigned)WS_KMAX + (nq * 256 + tt * 2) * 8 : (unsigned)WS_FLAG;
      const unsigned sh = w < 5 ? 20 : w == 5 ? 7 : 9, lim = w == 0 ? 16 : w < 5 ? 1 : w == 5 ? 7 : 3;
      unsigned long long pb = (unsigned long long)(((unsigned)(w - 1) < 4u) ? b14 : (const char*)ws + (w == 0 ? 12288u : c57));
      asm volatile("" : "+s"(pb));
      unsigned f0 = nq * 32 + tt, f1 = 256 + (j & 7) * 32 + xq * 4 + (j >> 3), f2 = 512 + xq * 32 + j;
      asm volatile("" : "+s"(f0), "+s"(f1), "+s"(f2));
      const unsigned fo = (lane == 0 ? f0 : lane == 1 ? f1 : lane == 2 ? f2 : f2 + 256) * FSTR;
      asm volatile("global_load_dword %0, %1, %2" : "=v"(pft) : "v"(w == 7 ? fo : min((unsigned)lane, lim) << sh), "s"(pb) : "memory");
    }
    const unsigned oa_ = (unsigned)((16 * w + g) * 1024 + ((t ^ (g << 1)) * 16)), ob_ = (unsigned)((16 * w + 4 + g) * 1024 + ((t ^ ((4 + g) << 1)) * 16));
    const u32x4 dw = {oa_, ob_ - 1024, oa_ + 8192 - 2048, ob_ + 8192 - 3072};
    const unsigned rsb = lds0 + 2 * 32768 + 1024, cpb = rsb + 4096, mrb = cpb + 4096 + w * 256;
    const int r32 = t + 16 * h, c = g & 1, rho = (r32 & 0x13) | ((r32 & 4) << 1) | ((r32 & 8) >> 1);
    u32x4 ea = {rsb + (hl * 64 + m * 32 + t) * 8, cpb + w * 192 + g * 16, cpb + w * 192 + 128 + t * 4, rsb + (m * 32 + r) * 8};
    u32x4 eb = {cpb + (w ^ 1) * 192 + g * 16, cpb + (w ^ 1) * 192 + 128 + t * 4, mrb + r * 8, mrb + g * 32};
    u32x4 ec = {(unsigned)(c * 32 + r32) * 16, (unsigned)(h * 2 + c) * 256 + t * 16, (unsigned)(c * 32 + rho) * 16, (unsigned)h * 4};
    unsigned wt_ = (unsigned)(j * 64 + w * 8 + (lane & 7)) * 128, wtd_;
    asm volatile("" : "+{v[152:155]}"(ea), "+{v[156:159]}"(eb), "+{v[160:163]}"(ec), "+{v165}"(wt_));
    asm volatile("s_waitcnt vmcnt(6)\n\ts_barrier" ::: "memory");
    asm volatile(P1_LOOP : "={v164}"(wtd_), "={v[0:15]}"(A), "={v[16:31]}"(B), "={v[32:35]}"(C), "={v[80:87]}"(SX), "={v[96:103]}"(SQ), "+{v88}"(xa0), "+{v89}"(xa1), "+{v90}"(wa),
                   "+{s88}"(kc), "+{s89}"(kx), "+{s90}"(kw), "+{s[92:93]}"(gbo), "+v"(tag), "+v"(pfc), "+v"(pft)
                 : "{v[36:39]}"(ones8()), "{v92}"(dvx), "{v93}"(dvw), "{v[104:107]}"(dw), "{v165}"(wt_), [wo2] "s"(wo + 256 * D), [lw] "s"(lw), [keep] "s"(keep), "{s[80:83]}"(rx), "{s[84:87]}"(rw),
                   [m] "s"(m), [hl] "s"(hl) : "memory", "scc", CLOB_40_79, CLOB_112_151);
    tag = ((u64)(unsigned)__builtin_amdgcn_readfirstlane((unsigned)(tag >> 32)) << 32 | (unsigned)__builtin_amdgcn_readfirstlane((unsigned)tag)) + 1;
    const unsigned tile = (unsigned)((nq * 4 + hl) * 64 + tt * 2 + m);
    asm volatile(P1_EPI : "+{v164}"(wtd_), "+{v[0:15]}"(A), "+{v[16:31]}"(B), "+{v[32:35]}"(C), "+{v[80:87]}"(SX), "+{v[96:103]}"(SQ)
                 : "{v[152:155]}"(ea), "{v[156:159]}"(eb), "{v[160:163]}"(ec), [rs] "s"(wsd), [tb] "s"(tile * 1024), [ko] "s"((unsigned)WS_KMAX + tile * 8)
                 : "memory", "scc", "vcc", "s88", "s89", "v40", "v41", "v42", "v43", "v44", "v45", "v46", "v47", "v48", "v49", "v50", "v51", "v52", "v53", "v54", "v55", "v56", "v58", "v59",
                   "v60", "v61", "v62", "v63", "v64", "v65", "v66", "v67", "v68", "v69", "v70", "v71", "v72", "v73", "v74", "v75");
    if (w == 0) publish(ws, 0, nq, tt, tag, lane);
#pragma unroll 1
    for (int c = 1; c < 3; ++c, gbo += 131072)
      asm volatile("s_mov_b32 %0, m0\n\ts_mov_b32 m0, %6\n\ts_nop 0\n\tglobal_load_lds_dwordx4 %1, %5\n\tglobal_load_lds_dwordx4 %2, %5 offset:1024\n\tglobal_load_lds_dwordx4 %3, %5 offset:2048\n\t"
                   "global_load_lds_dwordx4 %4, %5 offset:3072\n\ts_mov_b32 m0, %0" : "=&s"(keep) : "v"(dw[0]), "v"(dw[1]), "v"(dw[2]), "v"(dw[3]), "s"(gbo), "s"(lw + c * 32768) : "memory");
#define WO_READ(W, base) do { const lptr oa_ = (lptr)smem + (base) + (16 * (g >> 1) + 4 * (g & 1) + q) * 256 + ((w ^ (4 * (g & 1) + q)) * 32) + p * 8; \
      _Pragma("unroll") for (int s_ = 0; s_ < 8; ++s_) { const u32x4 f_ = __builtin_bit_cast(u32x4, trd2(oa_ + s_ * 8192, oa_ + s_ * 8192 + 2048)); \
        _Pragma("unroll") for (int i_ = 0; i_ < 4; ++i_) W[4 * s_ + i_] = f_[i_]; } } while (0)
    WO_READ(wo0, 3 * 32768);
    asm volatile("" : "+{v[192:223]}"(wo0));
  }
  const int G0 = j < 22 ? 3 * j : 66 + 2 * (j - 22); const bool three = j < 22;
  u32x2 xr0, xr1;
#define DOFF const unsigned doff = (unsigned)((lane >> 3) * 1024 + (((lane & 7) ^ ((g & 1) << 2)) * 16))
  const char* wdb = (const char*)wd + (j & 7) * 128;
#define WD_ROW(i, k2) ((((k2) >> 1) * 22 - ((k2) >= 6) + (i)) * 16 + ((k2) & 1) * 8)
  u32x32 wd0; f32x16 wd1; u32x4 wd2, wd3, wd4;
#define WD_REGS(m) m "{v[128:159]}"(wd0), m "{v[160:175]}"(wd1), m "{v[176:179]}"(wd2), m "{v[180:183]}"(wd3), m "{v[184:187]}"(wd4)
  const u32x4 wdd = {(unsigned)(size_t)wdb, (unsigned)((size_t)wdb >> 32) & 0xffffu, (unsigned)(FF * D * 2 - (j & 7) * 128), 0x00020000u};
  {
    LANES;
    const int head = xq * 4 + (j >> 3), qt = j & 7, kh = w >> 2, T0 = qt * 8 + (w & 3) * 2;
    const char* kg = ws + WS_K + (size_t)head * 65536 + (kh * 32 + (w & 3)) * 1024;
    const unsigned kl = lds0 + w * 1024;
    const bool vl = (lane & 16) == 0;
    unsigned kp = lds0 + kh * 4096 + lane * 16, vp = lds0 + (vl ? 8192 + kh * 4096 : L_ONE) + h * 256 + t * 16, vs = vl ? 16384u : 0u, l16 = lane * 16;
    unsigned pfo = (unsigned)min(j * 344 + w * 43 + lane, 11007) * 128;
#define GO_INIT unsigned go[3]; _Pragma("unroll") for (int n = 0; n < 3; ++n) { const int o = n * 1024 + 16 * lane, row = o / 96; int c6 = (o % 96) >> 4; if (!three) c6 &= 3; go[n] = (unsigned)(row * 2752 + G0 * 32 + c6 * 16); }
    unsigned pco; { unsigned len; asm volatile("s_mov_b32 %0, .Lfb_end-.Lfb_begin" : "=s"(len)); pco = min((unsigned)(j * 8 + w) * 128u, len) & ~3u; }
    unsigned xo = (unsigned)(t * D + w * 16 + 4 * g) * 2; const u16* xb = x + (size_t)P3T * 32 * D + P3C * 128;
    GO_INIT; DOFF; unsigned dof = doff, wo_ = (unsigned)WD_ROW(5, w) * 1024;
    f32x16 o0, o1, sb; u32x4 v1; int idx;
    unsigned flw = lds0 + L_SCR + w * 4, flr = lds0 + L_SCR, woa = lds0 + 32768 + (16 * (g >> 1) + 4 * (g & 1) + q) * 256 + ((w ^ (4 * (g & 1) + q)) * 32) + p * 8;
    asm volatile("" : "+{v101}"(kp), "+{v102}"(vp), "+{v107}"(vs), "+{v105}"(pfo), "+{v106}"(dof), "+{v116}"(flw), "+{v117}"(flr), "+{v118}"(woa), "+{v122}"(go[0]), "+{v123}"(go[1]), "+{v124}"(go[2]), "+{v121}"(l16), "+{v125}"(pco), "+{v126}"(xo));
    if (w == 0) poll(ws, 0, xq, tag, lane);
    LGKM_BAR();
    u32x4 q0, q1; u32x2 kmx;
    asm volatile("global_load_dwordx4 %0, %3, %4 sc1\n\tglobal_load_dwordx4 %1, %3, %4 offset:1024 sc1\n\tglobal_load_dwordx2 %2, %5, %6 sc1" : "=&{v[80:83]}"(q0), "=&{v[84:87]}"(q1), "=&{v[112:113]}"(kmx)
                 : "v"(l16), "s"(ws + WS_Q + (size_t)(head * 64 + T0) * 1024), "v"(l16 >> 1), "s"(ws + WS_KMAX + head * 512) : "memory");
#define AT_ISSUE(st) do { dma16_sc1(l16, kg + (st) * 4096, kl + (st) * 16384); dma16_sc1(l16, kg + (WS_V - WS_K) + (st) * 4096, kl + 8192 + (st) * 16384); } while (0)
    AT_ISSUE(0); AT_ISSUE(1);
    if (w < 4) { const unsigned ov = (lane & 11) ? 0u : 0x3f803f80u; *(LAS u32x4*)((lptr)smem + L_ONE + w * 1024 + l16) = (u32x4){ov, ov, ov, ov}; }
    float l0, l1;
    asm volatile(ZERO_STATE : "={v[0:15]}"(o0), "={v[16:31]}"(o1), "={v[64:79]}"(sb), "={v[96:99]}"(v1), "={v100}"(idx), "={v119}"(l0), "={v120}"(l1));
    f32x16 negm, sa; u32x4 kf, v0; unsigned slow;
    asm volatile(AT_PRO : "={v[32:47]}"(negm), "={v[48:63]}"(sa), "={v[88:91]}"(kf), "={v[92:95]}"(v0), "+{v[112:113]}"(kmx), "+{v[80:83]}"(q0), "+{v[84:87]}"(q1), [slow] "=&s"(slow)
                 : "{v101}"(kp), "{v102}"(vp), "{v116}"(flw), "{v117}"(flr) : "memory", "vcc", "scc", "v108", "v109", "v110", "v111", "v114", "v115");
    unsigned vn = 0, keep;
#define AT_OUT "+{v[0:15]}"(o0), "+{v[16:31]}"(o1), "+{v[48:63]}"(sa), "+{v[64:79]}"(sb), "+{v[88:91]}"(kf), "+{v[92:95]}"(v0), "+{v[96:99]}"(v1), "+{v101}"(kp), "+{v102}"(vp), "+{v103}"(vn), \
               "+{v[192:223]}"(wo0), "+{v119}"(l0), "+{v120}"(l1)
#define AT_IN "{v118}"(woa), "{v126}"(xo), [x0] "s"(xb), [x1] "s"(xb + 16 * D), "{v[32:47]}"(negm), "{v[80:83]}"(q0), "{v[84:87]}"(q1), "{v100}"(idx), "{v107}"(vs)
    const char* gwb = (const char*)wg + 2 * w * (32 * 2752); const unsigned gl = lds0 + L_WG + 2 * w * 3072;
    {
      const char* ks = kg + 2 * 4096; const char* vsr = ks + (WS_V - WS_K); unsigned c_, t_;
      asm volatile(AT_SLOW : AT_OUT, WD_REGS("="), "={v[224:255]}"(wo1), "={v[188:189]}"(xr0), "={v[190:191]}"(xr1), [wo] "+s"(wo_), "+{s[88:89]}"(ks), "+{s[90:91]}"(vsr), [keep] "=&s"(keep), [c] "=&s"(c_), [t] "=&s"(t_)
                   : AT_IN, "{v106}"(dof), "{v121}"(l16), "{v122}"(go[0]), "{v123}"(go[1]), "{v124}"(go[2]), [wr] "s"(wdd), [slow] "s"(slow), [kl] "s"(kl), [gl] "s"(gl), [g0] "s"(gwb), [g1] "s"(gwb + 32 * 2752)
                   : "memory", "scc", "v108", "v109");
    }
    asm volatile(AT_FAST : AT_OUT, WD_REGS("+"), "+{v[224:255]}"(wo1), "+{v[188:189]}"(xr0), "+{v[190:191]}"(xr1), [wo] "+s"(wo_), [keep] "=&s"(keep)
                 : AT_IN, "{v105}"(pfo), "{v106}"(dof), "{v121}"(l16), "{v122}"(go[0]), "{v123}"(go[1]), "{v124}"(go[2]), "{v125}"(pco), [rs] "s"(wsd), [wr] "s"(wdd), [wu] "s"(wu), [wd] "s"(wd),
                   [slow] "s"(slow), [kl] "s"(kl), [ko] "s"((unsigned)WS_K + head * 65536 + (kh * 32 + (w & 3)) * 1024), [gl] "s"(gl), [g0] "s"(gwb), [g1] "s"(gwb + 32 * 2752)
                 : "memory", "scc", "v104", "s88", "s89");
#undef AT_OUT
#undef AT_IN
#undef AT_ISSUE
    asm volatile(AT_COMB : "+{v[0:15]}"(o0), "+{v[16:31]}"(o1), "+{v119}"(l0), "+{v[192:223]}"(wo0), "+{v[224:255]}"(wo1), WD_REGS("+")
                 : "{v120}"(l1), "{v108}"(lds0 + L_O + w * 3072 + l16), "{v109}"(lds0 + L_O + (w ^ 4) * 3072 + l16), "{v121}"(l16), [kh] "s"(kh), [rs] "s"(wsd), [oo] "s"((unsigned)WS_O + ((T0 + kh) * 32 + head) * 1024)
                 : "memory", "scc", "v32", "v33", "v34", "v35", "v36", "v37", "v38", "v39", "v40", "v41", "v42", "v43", "v44", "v45");
    if (w == 0) publish(ws, 1, qt, head, tag, lane);
  }
#define GWA ((lptr)smem + ((g & 1) ? L_WU : L_WG) + (4 * h + q) * 96 + p * 8)
  LAS float* cl = (LAS float*)((lptr)smem + L_SCR + 8192);
  {
    LANES;
    {
      GO_INIT;
#pragma unroll 1
      for (int u = 2 * w; u < 2 * w + 2; ++u) { const char* src = (const char*)wu + u * (32 * 2752); const unsigned dst = lds0 + L_WU + u * 3072;
        dma16(go[0], src, dst); dma16(go[1], src, dst + 1024); dma16(go[2], src, dst + 2048); }
    }
    f32x4 c0 = {bflo(xr0[0]), bfhi(xr0[0]), bflo(xr0[1]), bfhi(xr0[1])}, c1 = {bflo(xr1[0]), bfhi(xr1[0]), bflo(xr1[1]), bfhi(xr1[1])};
    unsigned l16 = lane * 16, oaddr = lds0 + L_O + (g >> 1) * 1024 + (g & 1) * 512 + t * 16, hoff = ((g & 1) * 32 + t + 16 * h) * 16;
    unsigned sca = lds0 + L_SCR + (w * 64 + (16 * (g >> 1) + t) * 2 + (g & 1)) * 4;
    asm volatile("" : "+{v[0:3]}"(c0), "+{v[4:7]}"(c1), "+v"(l16), "+{v104}"(oaddr), "+{v105}"(hoff), "+{v106}"(sca), WD_REGS("+"));
    if (w == 0) poll(ws, 1, xq, tag, lane);
    LGKM_BAR();
    const char* og = ws + WS_O + (size_t)(P3T * 32 + w * 2) * 1024;
    const unsigned ol = lds0 + L_O + w * 2048;
    dma16_sc1(l16, og, ol); dma16_sc1(l16, og + 1024, ol + 1024); dma16_sc1(l16, og + 16384, ol + 16384); dma16_sc1(l16, og + 17408, ol + 17408);
    asm volatile(P3_BODY : "+{v[0:3]}"(c0), "+{v[4:7]}"(c1), WD_REGS("+") : "{v[192:223]}"(wo0), "{v[224:255]}"(wo1), "{v104}"(oaddr), "{v105}"(hoff), "{v106}"(sca),
                 [rs] "s"(wsd), [ho] "s"((unsigned)WS_H + (P3T * 32 + P3C * 8 + w) * 1024) : "memory", CLOB_8_103);
    LAS float* sc = (LAS float*)((lptr)smem + L_SCR);
    if (w == 0) {
      float v = 0.f;
#pragma unroll
      for (int i = 0; i < 8; ++i) v += sc[i * 64 + lane];
      __builtin_amdgcn_raw_buffer_store_b32(__float_as_uint(v), wsr, WS_STAT + ((P3T * 32 + (lane >> 1)) * 8 + P3C * 2 + (lane & 1)) * 4, 0, 16);
    }
    WAIT_BAR(0);
    if (w == 0) publish(ws, 2, xq, j, tag, lane);
    if (w >= 5) {
      f32x16 cs;
      asm volatile(COLSUM : "={v[0:15]}"(cs) : "{v[16:19]}"(ones8()), "{v48}"((unsigned)(size_t)GWA + (w - 5) * 32)
                   : "memory", "v20", "v21", "v22", "v23", "v24", "v25", "v26", "v27", "v28", "v29", "v30", "v31", "v32", "v33", "v34", "v35", "v36", "v37", "v38", "v39", "v40", "v41", "v42", "v43", "v44", "v45", "v46", "v47");
      if (r == 0) { LAS f32x4* d = (LAS f32x4*)(cl + ((w - 5) * 2 + h) * 16); d[0] = q4<0>(cs); d[1] = q4<1>(cs); d[2] = q4<2>(cs); d[3] = q4<3>(cs); }
    }
  }
  u32x4 hres;
  {
    LANES;
    const int T = xq * 8 + w;
    unsigned so = (unsigned)WS_H + T * 32768, gwa = (unsigned)(size_t)GWA, l16 = lane * 16, sta = (unsigned)WS_STAT + (T * 32 + r) * 32;
    unsigned cla = (unsigned)(size_t)cl + h * 64, keep, eo = (unsigned)WD_ROW(0, w) * 1024; DOFF; unsigned dof = doff;
    asm volatile("" : "+{v72}"(gwa), "+{v73}"(dof), "+{v74}"(l16), "+{v75}"(sta), "+{v124}"(cla), WD_REGS("+"));
    if (w == 0) poll(ws, 2, xq, tag, lane);
    LGKM_BAR();
    asm volatile(P4_LOOP : WD_REGS("+"), "={v[244:247]}"(hres), [so] "+s"(so), [eo] "+s"(eo), [keep] "=&s"(keep)
                 : "{v72}"(gwa), "{v73}"(dof), "{v74}"(l16), "{v75}"(sta), "{v124}"(cla), [wr] "s"(wdd), [el] "s"(lds0 + 13 * 8192 + w * 1024), [three] "s"((j - 22) >> 31), [rs] "s"(wsd), [ao] "s"((unsigned)WS_ACT + (T * 86 + G0) * 1024),
                   [ro] "s"((unsigned)WS_H + ((xq * 8 + (j >> 3) * 2 + (w & 1)) * 32 + (j & 7) * 4 + (w >> 1)) * 1024)
                 : "memory", "scc", "vcc", CLOB_0_71, CLOB_76_123, CLOB_188_243);
    asm volatile("s_waitcnt vmcnt(0) lgkmcnt(0)\n\ts_barrier" : WD_REGS("+") :: "memory");
    if (w == 0) publish(ws, 3, xq, j, tag, lane);
    { unsigned wa_ = lds0 + w * 1024 + l16; asm volatile(WD_WRITE : "+{v48}"(wa_) : WD_REGS("") : "memory"); }
  }
  {
    LANES; DOFF;
    const int T = xq * 8 + (j >> 3) * 2 + (w & 1), cn = j & 7, kq = w >> 1;
    unsigned wa0 = lds0 + 13 * 8192 + kq * 2048 + (4 * h + q) * 128 + ((q >> 1) & 1) * 64 + (g & 1) * 32 + p * 8, wa1 = wa0 ^ 64;
    unsigned so = (unsigned)WS_ACT + (T * 86 + kq * 22 - (kq == 3)) * 1024, keep, l16 = lane * 16, dof = doff;
    f32x16 acc0, acc1;
#pragma unroll
    for (int i = 0; i < 8; ++i) { const float v = (i & 1) ? bfhi(hres[i >> 1]) : bflo(hres[i >> 1]);
      acc0[i] = kq == 0 ? v : 0.f; acc0[8 + i] = kq == 1 ? v : 0.f; acc1[i] = kq == 2 ? v : 0.f; acc1[8 + i] = kq == 3 ? v : 0.f; }
    const int tok = 8 * w + (lane >> 3);
    unsigned ea = lds0 + (kq * 64 + (w & 1) * 32 + r) * 256 + ((h ^ t) * 16), eb = lds0 + tok * 256 + (((2 * (lane & 7)) ^ (tok & 15)) * 16);
    unsigned oo = (((xq * 4 + (j >> 3)) * 64 + tok) * D + cn * 64 + (lane & 7) * 8) * 2;
    asm volatile("" : "+{v[0:15]}"(acc0), "+{v[16:31]}"(acc1), "+{v48}"(wa0), "+{v49}"(wa1), "+{v52}"(l16), "+{v53}"(dof), "+{v54}"(ea), "+{v55}"(eb), "+{v56}"(oo));
    if (w == 0) poll(ws, 3, xq, tag, lane);
    LGKM_BAR();
    asm volatile(P5_LOOP : "+{v[0:15]}"(acc0), "+{v[16:31]}"(acc1), "+{v48}"(wa0), "+{v49}"(wa1), [so] "+s"(so), [keep] "=&s"(keep)
                 : "{v52}"(l16), "{v53}"(dof), [rs] "s"(wsd), [sh] "s"((w >> 2) * 1024), [l0] "s"(lds0 + 13 * 8192 + w * 1024),
                   [g0] "s"(wdb + (size_t)WD_ROW(20, w) * 1024), [g1] "s"(wdb + (size_t)WD_ROW(21, w & 3) * 1024)
                 : "memory", "scc", "v32", "v33", "v34", "v35", "v36", "v37", "v38", "v39", "v40", "v41", "v42", "v43", "v44", "v45", "v46", "v47", CLOB_128_255);
#define EXW(c, v) *(LAS f32x4*)(size_t)(ea ^ ((c) * 32)) = (v)
    EXW(0, q4<0>(acc0)); EXW(1, q4<1>(acc0)); EXW(2, q4<2>(acc0)); EXW(3, q4<3>(acc0)); EXW(4, q4<0>(acc1)); EXW(5, q4<1>(acc1)); EXW(6, q4<2>(acc1)); EXW(7, q4<3>(acc1));
#undef EXW
    const lptr e0 = (lptr)(size_t)eb, e1 = (lptr)(size_t)(eb ^ 16);
    LGKM_BAR();
    const f32x4 y0 = (*(LAS f32x4*)e0 + *(LAS f32x4*)(e0 + 16384)) + (*(LAS f32x4*)(e0 + 32768) + *(LAS f32x4*)(e0 + 49152));
    const f32x4 y1 = (*(LAS f32x4*)e1 + *(LAS f32x4*)(e1 + 16384)) + (*(LAS f32x4*)(e1 + 32768) + *(LAS f32x4*)(e1 + 49152));
    __builtin_amdgcn_raw_buffer_store_b128((u32x4){pk(y0[0], y0[1]), pk(y0[2], y0[3]), pk(y1[0], y1[1]), pk(y1[2], y1[3])}, __builtin_amdgcn_make_buffer_rsrc(out, 0, S * D * 2, 0x00020000), oo, 0, 0);
    if (b == 0 && w == 0 && lane == 0) __hip_atomic_store((gu64*)(ws + WS_EPOCH), tag, RLX_AGENT);
  }
  asm volatile(".Lfb_end:");
}

extern "C" void kernel_launch(void* const* d_in, const int* in_sizes, int n_in, void* d_out, int out_size, void* d_ws, size_t ws_size, hipStream_t stream) {
  (void)hipFuncSetAttribute((const void*)fused_block, hipFuncAttributeMaxDynamicSharedMemorySize, LDS_BYTES);
  fused_block<<<256, 512, LDS_BYTES, stream>>>((const u16*)d_in[0], (const u16*)d_in[1], (const u16*)d_in[2], (const u16*)d_in[4], (const u16*)d_in[5], (const u16*)d_in[6], (u16*)d_out, (char*)d_ws);
}
```
